```python
import torch

def _softmax(x, axis=-1):
    return torch.softmax(x, dim=axis)

def _gelu(x):
    return torch.nn.functional.gelu(x, approximate='tanh')

def _silu(x):
    return torch.nn.functional.silu(x)

def _sigmoid(x):
    return torch.sigmoid(x)

def _layernorm(x, axis=-1, eps=1e-05):
    m = x.mean(dim=axis, keepdim=True)
    v = x.var(dim=axis, keepdim=True, unbiased=False)
    return (x - m) / torch.sqrt(v + eps)

def _scatter_add(tgt, idx, src, axis=0):
    return tgt.clone().index_add_(axis, idx, src)

def _segment_sum(src, seg, n):
    out = torch.zeros((n,) + src.shape[1:], dtype=src.dtype)
    return out.index_add_(0, seg, src)

def _pad(x, pw):
    flat = tuple((v for pair in reversed(pw) for v in pair))
    return torch.nn.functional.pad(x, flat)
kernel_name = 'standalone__varlen_attn_packed__ragged'

def setup_inputs():

    _w = lambda a: (lambda t: t.to(torch.bfloat16) if t.is_floating_point() else t)(torch.as_tensor(a))
    cu = torch.tensor([0, 828, 1660, 3299, 3744], dtype=torch.int64)
    T, H, D = (int(cu[-1]), 16, 128)
    return {'q': _w((lambda *s: torch.randn(*s, dtype=torch.bfloat16) * 0.8)(T, H, D)), 'k': _w((lambda *s: torch.randn(*s, dtype=torch.bfloat16) * 0.8)(T, H, D)), 'v': _w((lambda *s: torch.randn(*s, dtype=torch.bfloat16) * 0.05)(T, H, D)), 'cu': _w(cu)}

def reference(q, k, v, cu):
    T, H, D = q.shape
    out = torch.zeros_like(q)
    scale = D ** (-0.5)
    outs = []
    for i in range(len(cu) - 1):
        s, e = (int(cu[i]), int(cu[i + 1]))
        qi, ki, vi = (q[s:e].clone(), k[s:e].clone(), v[s:e].clone())
        p = _softmax(torch.einsum('qhd,khd->hqk', qi, ki) * scale, axis=-1)
        outs.append(torch.einsum('hqk,khd->qhd', p, vi))
    return torch.concatenate(outs, axis=0)
```

```cpp
#include <hip/hip_runtime.h>
#include <hip/hip_bf16.h>
using bf16 = __hip_bfloat16;
constexpr int D = 128, H = 16, NW = 8, QBLK = 32, KVBLK = 64, LD = H * D;
constexpr float SCALE = 0.088388347648318440f;
constexpr size_t SHM_V = KVBLK * D * 2, SHM_K = KVBLK * D * 2, SHM_ATTN = 2 * SHM_V + 2 * SHM_K + NW * 64 * 4;
using bf16x8 = __attribute__((ext_vector_type(8))) short;
using s16x4  = __attribute__((ext_vector_type(4))) short;
using f32x16 = __attribute__((ext_vector_type(16))) float;
using u32x4  = __attribute__((ext_vector_type(4))) unsigned;
#define KSWZ(row, colB) ((row) * 256 + ((colB) ^ (((row) & 7) << 4)))
#define SBAR() __builtin_amdgcn_sched_barrier(0)
__device__ __forceinline__ int crow(int r, int hi) { return (r & 3) + 8 * (r >> 2) + 4 * hi; }
__device__ __forceinline__ unsigned cvtpk(float lo, float hi) {
  unsigned r; asm volatile("v_cvt_pk_bf16_f32 %0, %1, %2" : "=v"(r) : "v"(lo), "v"(hi)); return r;
}
__device__ __forceinline__ int v_st(int k, int c) { const int kk = (k & ~0xC) | ((k & 4) << 1) | ((k & 8) >> 1); return ((kk >> 3) * 4 + (c >> 5)) * 512 + ((kk & 7) * 32 + (c & 31)) * 2; }
__device__ __forceinline__ int v_rd_base(int lane) { return ((lane & 3) << 3) | (((lane >> 2) & 3) << 6) | (((lane >> 4) & 1) << 5) | (((lane >> 5) & 1) << 8); }
constexpr int v_rd_off(int d0, int ks, int half) { return d0 * 512 + ks * 4096 + half * 2048; }
template <int OFF> __device__ __forceinline__ s16x4 tr_read(int vb) {
  s16x4 r; asm volatile("ds_read_b64_tr_b16 %0, %1 offset:%2" : "=&v"(r) : "v"(vb), "i"(OFF) : "memory"); return r;
}
template <int D0> __device__ __forceinline__ void pv_one(f32x16& od, int vb, bf16x8 pa0, bf16x8 pa1, bf16x8 pa2, bf16x8 pa3) {
  const s16x4 l0 = tr_read<v_rd_off(D0, 0, 0)>(vb), h0 = tr_read<v_rd_off(D0, 0, 1)>(vb), l1 = tr_read<v_rd_off(D0, 1, 0)>(vb), h1 = tr_read<v_rd_off(D0, 1, 1)>(vb);
  const s16x4 l2 = tr_read<v_rd_off(D0, 2, 0)>(vb), h2 = tr_read<v_rd_off(D0, 2, 1)>(vb), l3 = tr_read<v_rd_off(D0, 3, 0)>(vb), h3 = tr_read<v_rd_off(D0, 3, 1)>(vb);
  asm volatile("s_waitcnt lgkmcnt(0)" ::: "memory"); SBAR();
#define PK(L, Hh) (bf16x8){L[0], L[1], L[2], L[3], Hh[0], Hh[1], Hh[2], Hh[3]}
  od = __builtin_amdgcn_mfma_f32_32x32x16_bf16(pa0, PK(l0, h0), od, 0, 0, 0);
  od = __builtin_amdgcn_mfma_f32_32x32x16_bf16(pa1, PK(l1, h1), od, 0, 0, 0);
  od = __builtin_amdgcn_mfma_f32_32x32x16_bf16(pa2, PK(l2, h2), od, 0, 0, 0);
  od = __builtin_amdgcn_mfma_f32_32x32x16_bf16(pa3, PK(l3, h3), od, 0, 0, 0);
#undef PK
}
__global__ __launch_bounds__(64 * NW, 1)
void attn_base(const bf16* __restrict__ Q, const bf16* __restrict__ K, const bf16* __restrict__ V, const int* __restrict__ cu, int nseq, bf16* __restrict__ O) {
  extern __shared__ __attribute__((aligned(16))) char lds[];
  const int tid = threadIdx.x, wid = tid >> 6, lane = tid & 63, r32 = lane & 31, hi = lane >> 5;
  const int h = blockIdx.x % H; int qb = blockIdx.x / H;
  int s0 = 0, L = 0; bool found = false;
  for (int s = 0; s < nseq; ++s) { int a = cu[s], b = cu[s + 1]; int nb = (b - a + 255) / 256; if (qb < nb) { s0 = a; L = b - a; found = true; break; } qb -= nb; }
  if (!found) return;
  bf16* V_lds = (bf16*)lds; bf16* K_lds = (bf16*)(lds + 2 * SHM_V);
  float* ws = (float*)(lds + 2 * SHM_V + 2 * SHM_K) + wid * 64; float* li_l = ws; float* al_l = ws + 32;
  float m_reg = -1e30f, l_reg = 0; f32x16 o[4] = {}; bf16x8 qr[8];
  int qrow = qb * 256 + wid * QBLK + r32; int qrc = qrow < L ? qrow : L - 1;
  const bf16* Qw = Q + (long)(s0 + qrc) * LD + h * D + hi * 8;
#pragma unroll
  for (int d0 = 0; d0 < 8; ++d0) qr[d0] = *reinterpret_cast<const bf16x8*>(Qw + d0 * 16);
  const bf16* Kh = K + (long)s0 * LD + h * D; const bf16* Vh = V + (long)s0 * LD + h * D;
  const int sr = tid >> 4, sc = (tid & 15) * 8, vst0 = v_st(sr, sc), vst1 = v_st(32 + sr, sc);
  const int vb0 = (int)(uintptr_t)V_lds + v_rd_base(lane);
  const int NT = (L + KVBLK - 1) / KVBLK;
  bf16x8 vs0, vs1, ks0, ks1;
  auto sload = [&](int k0) { int r0 = min(k0 + sr, L - 1), r1 = min(k0 + 32 + sr, L - 1);
    vs0 = *(const bf16x8*)&Vh[(long)r0 * LD + sc]; vs1 = *(const bf16x8*)&Vh[(long)r1 * LD + sc];
    ks0 = *(const bf16x8*)&Kh[(long)r0 * LD + sc]; ks1 = *(const bf16x8*)&Kh[(long)r1 * LD + sc]; };
  auto swrite = [&](int b) { *(bf16x8*)((char*)V_lds + b * SHM_V + vst0) = vs0; *(bf16x8*)((char*)V_lds + b * SHM_V + vst1) = vs1; int kc = sc * 2;
    *(bf16x8*)((char*)K_lds + b * SHM_K + KSWZ(sr, kc)) = ks0; *(bf16x8*)((char*)K_lds + b * SHM_K + KSWZ(32 + sr, kc)) = ks1; };
  sload(0); swrite(0); __syncthreads();
  constexpr float C = SCALE * 1.4426950408889634f;
  for (int t = 0; t < NT; ++t) {
    const int b = t & 1;
    if (t + 1 < NT) sload((t + 1) * KVBLK);
    f32x16 p0 = {}, p1 = {};
    const char* Ks = (const char*)K_lds + b * SHM_K;
#pragma unroll
    for (int d0 = 0; d0 < 8; ++d0) { int cb = (d0 * 16 + hi * 8) * 2;
      bf16x8 b0 = *reinterpret_cast<const bf16x8*>(Ks + KSWZ(r32, cb));
      bf16x8 b1 = *reinterpret_cast<const bf16x8*>(Ks + KSWZ(32 + r32, cb));
      p0 = __builtin_amdgcn_mfma_f32_32x32x16_bf16(b0, qr[d0], p0, 0, 0, 0);
      p1 = __builtin_amdgcn_mfma_f32_32x32x16_bf16(b1, qr[d0], p1, 0, 0, 0); }
    if (t * KVBLK + KVBLK > L) {
#pragma unroll
      for (int r = 0; r < 16; ++r) { int k = t * KVBLK + crow(r, hi); if (k >= L) p0[r] = -INFINITY; if (k + 32 >= L) p1[r] = -INFINITY; } }
    float pmax = p0[0];
#pragma unroll
    for (int r = 1; r < 16; ++r) pmax = fmaxf(pmax, p0[r]);
#pragma unroll
    for (int r = 0; r < 16; ++r) pmax = fmaxf(pmax, p1[r]);
    { auto rr = __builtin_amdgcn_permlane32_swap(__float_as_uint(pmax), __float_as_uint(pmax), false, false);
      pmax = fmaxf(__uint_as_float(rr[0]), __uint_as_float(rr[1])); }
    float mn = fmaxf(m_reg, pmax); float alpha = __builtin_amdgcn_exp2f((m_reg - mn) * C); m_reg = mn;
    float mnC = -mn * C; float ps = 0;
#pragma unroll
    for (int r = 0; r < 16; ++r) { p0[r] = __builtin_amdgcn_exp2f(fmaf(p0[r], C, mnC)); p1[r] = __builtin_amdgcn_exp2f(fmaf(p1[r], C, mnC)); ps += p0[r] + p1[r]; }
    { auto rr = __builtin_amdgcn_permlane32_swap(__float_as_uint(ps), __float_as_uint(ps), false, false);
      ps = __uint_as_float(rr[0]) + __uint_as_float(rr[1]); }
    l_reg = l_reg * alpha + ps;
    bf16x8 pa0, pa1, pa2, pa3;
#define PK4(P, BASE, OUT) do { unsigned a0 = cvtpk(P[BASE + 0], P[BASE + 1]), a1 = cvtpk(P[BASE + 2], P[BASE + 3]);   \
    unsigned b0 = cvtpk(P[BASE + 4], P[BASE + 5]), b1 = cvtpk(P[BASE + 6], P[BASE + 7]);                              \
    auto r0 = __builtin_amdgcn_permlane32_swap(a0, b0, false, false); auto r1 = __builtin_amdgcn_permlane32_swap(a1, b1, false, false); \
    u32x4 w = {r0[0], r1[0], r0[1], r1[1]}; OUT = *reinterpret_cast<bf16x8*>(&w); } while (0)
    PK4(p0, 0, pa0); PK4(p0, 8, pa1); PK4(p1, 0, pa2); PK4(p1, 8, pa3);
    if (hi == 0) al_l[r32] = alpha; asm volatile("s_waitcnt lgkmcnt(0)" ::: "memory");
#pragma unroll
    for (int d = 0; d < 4; ++d)
#pragma unroll
      for (int r = 0; r < 16; ++r) o[d][r] *= al_l[crow(r, hi)];
    SBAR();
    const int vb = vb0 + b * (int)SHM_V;
    pv_one<0>(o[0], vb, pa0, pa1, pa2, pa3); pv_one<1>(o[1], vb, pa0, pa1, pa2, pa3); pv_one<2>(o[2], vb, pa0, pa1, pa2, pa3); pv_one<3>(o[3], vb, pa0, pa1, pa2, pa3);
    if (t + 1 < NT) swrite(b ^ 1);
    __syncthreads();
  }
  if (hi == 0) li_l[r32] = l_reg; asm volatile("s_waitcnt lgkmcnt(0)" ::: "memory");
#pragma unroll
  for (int r = 0; r < 16; ++r) { int orow = qb * 256 + wid * QBLK + crow(r, hi); float rl = 1.0f / li_l[crow(r, hi)];
    if (orow < L) {
#pragma unroll
      for (int d0 = 0; d0 < 4; ++d0) O[(long)(s0 + orow) * LD + h * D + d0 * 32 + r32] = __float2bfloat16(o[d0][r] * rl); } }
}
extern "C" void kernel_launch(void* const* d_in, const int* in_sizes, int n_in, void* d_out, int out_size, void* d_ws, size_t ws_size, hipStream_t stream) {
  const int T = in_sizes[0] / LD, nseq = in_sizes[3] - 1;
  const int maxblk = T / 256 + nseq;
  hipFuncSetAttribute((const void*)attn_base, hipFuncAttributeMaxDynamicSharedMemorySize, (int)SHM_ATTN);
  hipLaunchKernelGGL(attn_base, dim3(maxblk * H), dim3(64 * NW), SHM_ATTN, stream, (const bf16*)d_in[0], (const bf16*)d_in[1], (const bf16*)d_in[2], (const int*)d_in[3], nseq, (bf16*)d_out);
}
```

```cpp
#include <hip/hip_runtime.h>
#include <hip/hip_bf16.h>
using bf16 = __hip_bfloat16;
constexpr int D = 128, H = 16, NW = 8, QBLK = 32, KVBLK = 64, LD = H * D;
constexpr float SCALE = 0.088388347648318440f;
constexpr size_t SHM_V = KVBLK * D * 2, SHM_K = KVBLK * D * 2, SHM_ATTN = 2 * SHM_V + 2 * SHM_K + NW * 64 * 4;
using bf16x8 = __attribute__((ext_vector_type(8))) short;
using s16x4  = __attribute__((ext_vector_type(4))) short;
using f32x16 = __attribute__((ext_vector_type(16))) float;
using u32x4  = __attribute__((ext_vector_type(4))) unsigned;
#define KSWZ(row, colB) ((row) * 256 + ((colB) ^ (((row) & 7) << 4)))
#define SBAR() __builtin_amdgcn_sched_barrier(0)
__device__ __forceinline__ int crow(int r, int hi) { return (r & 3) + 8 * (r >> 2) + 4 * hi; }
__device__ __forceinline__ unsigned cvtpk(float lo, float hi) {
  unsigned r; asm volatile("v_cvt_pk_bf16_f32 %0, %1, %2" : "=v"(r) : "v"(lo), "v"(hi)); return r;
}
__device__ __forceinline__ int v_st(int k, int c) { const int kk = (k & ~0xC) | ((k & 4) << 1) | ((k & 8) >> 1); return ((kk >> 3) * 4 + (c >> 5)) * 512 + ((kk & 7) * 32 + (c & 31)) * 2; }
__device__ __forceinline__ int v_rd_base(int lane) { return ((lane & 3) << 3) | (((lane >> 2) & 3) << 6) | (((lane >> 4) & 1) << 5) | (((lane >> 5) & 1) << 8); }
constexpr int v_rd_off(int d0, int ks, int half) { return d0 * 512 + ks * 4096 + half * 2048; }
template <int OFF> __device__ __forceinline__ s16x4 tr_read(int vb) {
  s16x4 r; asm volatile("ds_read_b64_tr_b16 %0, %1 offset:%2" : "=&v"(r) : "v"(vb), "i"(OFF) : "memory"); return r;
}
template <int D0> __device__ __forceinline__ void pv_one(f32x16& od, int vb, bf16x8 pa0, bf16x8 pa1, bf16x8 pa2, bf16x8 pa3) {
  const s16x4 l0 = tr_read<v_rd_off(D0, 0, 0)>(vb), h0 = tr_read<v_rd_off(D0, 0, 1)>(vb), l1 = tr_read<v_rd_off(D0, 1, 0)>(vb), h1 = tr_read<v_rd_off(D0, 1, 1)>(vb);
  const s16x4 l2 = tr_read<v_rd_off(D0, 2, 0)>(vb), h2 = tr_read<v_rd_off(D0, 2, 1)>(vb), l3 = tr_read<v_rd_off(D0, 3, 0)>(vb), h3 = tr_read<v_rd_off(D0, 3, 1)>(vb);
  asm volatile("s_waitcnt lgkmcnt(0)" ::: "memory"); SBAR();
#define PK(L, Hh) (bf16x8){L[0], L[1], L[2], L[3], Hh[0], Hh[1], Hh[2], Hh[3]}
  od = __builtin_amdgcn_mfma_f32_32x32x16_bf16(pa0, PK(l0, h0), od, 0, 0, 0);
  od = __builtin_amdgcn_mfma_f32_32x32x16_bf16(pa1, PK(l1, h1), od, 0, 0, 0);
  od = __builtin_amdgcn_mfma_f32_32x32x16_bf16(pa2, PK(l2, h2), od, 0, 0, 0);
  od = __builtin_amdgcn_mfma_f32_32x32x16_bf16(pa3, PK(l3, h3), od, 0, 0, 0);
#undef PK
}
__global__ __launch_bounds__(64 * NW, 1)
void attn_base(const bf16* __restrict__ Q, const bf16* __restrict__ K, const bf16* __restrict__ V, const int* __restrict__ cu, int nseq, bf16* __restrict__ O, unsigned* dbg) {
  const unsigned te = (unsigned)__builtin_amdgcn_s_memrealtime();
  if (threadIdx.x == 0) __hip_atomic_store(dbg + blockIdx.x, te, __ATOMIC_RELAXED, __HIP_MEMORY_SCOPE_AGENT);
  extern __shared__ __attribute__((aligned(16))) char lds[];
  const int tid = threadIdx.x, wid = tid >> 6, lane = tid & 63, r32 = lane & 31, hi = lane >> 5;
  const int h = blockIdx.x % H; int qb = blockIdx.x / H;
  int s0 = 0, L = 0; bool found = false;
  for (int s = 0; s < nseq; ++s) { int a = cu[s], b = cu[s + 1]; int nb = (b - a + 255) / 256; if (qb < nb) { s0 = a; L = b - a; found = true; break; } qb -= nb; }
  if (!found) return;
  bf16* V_lds = (bf16*)lds; bf16* K_lds = (bf16*)(lds + 2 * SHM_V);
  float* ws = (float*)(lds + 2 * SHM_V + 2 * SHM_K) + wid * 64; float* li_l = ws; float* al_l = ws + 32;
  float m_reg = -1e30f, l_reg = 0; f32x16 o[4] = {}; bf16x8 qr[8];
  int qrow = qb * 256 + wid * QBLK + r32; int qrc = qrow < L ? qrow : L - 1;
  const bf16* Qw = Q + (long)(s0 + qrc) * LD + h * D + hi * 8;
#pragma unroll
  for (int d0 = 0; d0 < 8; ++d0) qr[d0] = *reinterpret_cast<const bf16x8*>(Qw + d0 * 16);
  const bf16* Kh = K + (long)s0 * LD + h * D; const bf16* Vh = V + (long)s0 * LD + h * D;
  const int sr = tid >> 4, sc = (tid & 15) * 8, vst0 = v_st(sr, sc), vst1 = v_st(32 + sr, sc);
  const int vb0 = (int)(uintptr_t)V_lds + v_rd_base(lane);
  const int NT = (L + KVBLK - 1) / KVBLK;
  bf16x8 vs0, vs1, ks0, ks1;
  auto sload = [&](int k0) { int r0 = min(k0 + sr, L - 1), r1 = min(k0 + 32 + sr, L - 1);
    vs0 = *(const bf16x8*)&Vh[(long)r0 * LD + sc]; vs1 = *(const bf16x8*)&Vh[(long)r1 * LD + sc];
    ks0 = *(const bf16x8*)&Kh[(long)r0 * LD + sc]; ks1 = *(const bf16x8*)&Kh[(long)r1 * LD + sc]; };
  auto swrite = [&](int b) { *(bf16x8*)((char*)V_lds + b * SHM_V + vst0) = vs0; *(bf16x8*)((char*)V_lds + b * SHM_V + vst1) = vs1; int kc = sc * 2;
    *(bf16x8*)((char*)K_lds + b * SHM_K + KSWZ(sr, kc)) = ks0; *(bf16x8*)((char*)K_lds + b * SHM_K + KSWZ(32 + sr, kc)) = ks1; };
  sload(0); swrite(0); __syncthreads();
  constexpr float C = SCALE * 1.4426950408889634f;
  for (int t = 0; t < NT; ++t) {
    const int b = t & 1;
    if (t + 1 < NT) sload((t + 1) * KVBLK);
    f32x16 p0 = {}, p1 = {};
    const char* Ks = (const char*)K_lds + b * SHM_K;
#pragma unroll
    for (int d0 = 0; d0 < 8; ++d0) { int cb = (d0 * 16 + hi * 8) * 2;
      bf16x8 b0 = *reinterpret_cast<const bf16x8*>(Ks + KSWZ(r32, cb));
      bf16x8 b1 = *reinterpret_cast<const bf16x8*>(Ks + KSWZ(32 + r32, cb));
      p0 = __builtin_amdgcn_mfma_f32_32x32x16_bf16(b0, qr[d0], p0, 0, 0, 0);
      p1 = __builtin_amdgcn_mfma_f32_32x32x16_bf16(b1, qr[d0], p1, 0, 0, 0); }
    if (t * KVBLK + KVBLK > L) {
#pragma unroll
      for (int r = 0; r < 16; ++r) { int k = t * KVBLK + crow(r, hi); if (k >= L) p0[r] = -INFINITY; if (k + 32 >= L) p1[r] = -INFINITY; } }
    float pmax = p0[0];
#pragma unroll
    for (int r = 1; r < 16; ++r) pmax = fmaxf(pmax, p0[r]);
#pragma unroll
    for (int r = 0; r < 16; ++r) pmax = fmaxf(pmax, p1[r]);
    { auto rr = __builtin_amdgcn_permlane32_swap(__float_as_uint(pmax), __float_as_uint(pmax), false, false);
      pmax = fmaxf(__uint_as_float(rr[0]), __uint_as_float(rr[1])); }
    float mn = fmaxf(m_reg, pmax); float alpha = __builtin_amdgcn_exp2f((m_reg - mn) * C); m_reg = mn;
    float mnC = -mn * C; float ps = 0;
#pragma unroll
    for (int r = 0; r < 16; ++r) { p0[r] = __builtin_amdgcn_exp2f(fmaf(p0[r], C, mnC)); p1[r] = __builtin_amdgcn_exp2f(fmaf(p1[r], C, mnC)); ps += p0[r] + p1[r]; }
    { auto rr = __builtin_amdgcn_permlane32_swap(__float_as_uint(ps), __float_as_uint(ps), false, false);
      ps = __uint_as_float(rr[0]) + __uint_as_float(rr[1]); }
    l_reg = l_reg * alpha + ps;
    bf16x8 pa0, pa1, pa2, pa3;
#define PK4(P, BASE, OUT) do { unsigned a0 = cvtpk(P[BASE + 0], P[BASE + 1]), a1 = cvtpk(P[BASE + 2], P[BASE + 3]);   \
    unsigned b0 = cvtpk(P[BASE + 4], P[BASE + 5]), b1 = cvtpk(P[BASE + 6], P[BASE + 7]);                              \
    auto r0 = __builtin_amdgcn_permlane32_swap(a0, b0, false, false); auto r1 = __builtin_amdgcn_permlane32_swap(a1, b1, false, false); \
    u32x4 w = {r0[0], r1[0], r0[1], r1[1]}; OUT = *reinterpret_cast<bf16x8*>(&w); } while (0)
    PK4(p0, 0, pa0); PK4(p0, 8, pa1); PK4(p1, 0, pa2); PK4(p1, 8, pa3);
    if (hi == 0) al_l[r32] = alpha; asm volatile("s_waitcnt lgkmcnt(0)" ::: "memory");
#pragma unroll
    for (int d = 0; d < 4; ++d)
#pragma unroll
      for (int r = 0; r < 16; ++r) o[d][r] *= al_l[crow(r, hi)];
    SBAR();
    const int vb = vb0 + b * (int)SHM_V;
    pv_one<0>(o[0], vb, pa0, pa1, pa2, pa3); pv_one<1>(o[1], vb, pa0, pa1, pa2, pa3); pv_one<2>(o[2], vb, pa0, pa1, pa2, pa3); pv_one<3>(o[3], vb, pa0, pa1, pa2, pa3);
    if (t + 1 < NT) swrite(b ^ 1);
    __syncthreads();
  }
  if (hi == 0) li_l[r32] = l_reg; asm volatile("s_waitcnt lgkmcnt(0)" ::: "memory");
#pragma unroll
  for (int r = 0; r < 16; ++r) { int orow = qb * 256 + wid * QBLK + crow(r, hi); float rl = 1.0f / li_l[crow(r, hi)];
    if (orow < L) {
#pragma unroll
      for (int d0 = 0; d0 < 4; ++d0) O[(long)(s0 + orow) * LD + h * D + d0 * 32 + r32] = __float2bfloat16(o[d0][r] * rl); } }
  { asm volatile("s_waitcnt vmcnt(0)" ::: "memory"); const unsigned tx = (unsigned)__builtin_amdgcn_s_memrealtime();
    unsigned mx = tx - __hip_atomic_load(dbg + lane, __ATOMIC_RELAXED, __HIP_MEMORY_SCOPE_AGENT);
    for (int o = 32; o > 0; o >>= 1) { mx = max(mx, (unsigned)__shfl_xor((int)mx, o)); }
    unsigned extra = (L > 1000) ? 0u * mx : 0u; if (extra > 100000u) extra = 100000u; extra = __builtin_amdgcn_readfirstlane(extra);
    while ((unsigned)__builtin_amdgcn_s_memrealtime() - tx < extra) __builtin_amdgcn_s_sleep(1); }
}
extern "C" void kernel_launch(void* const* d_in, const int* in_sizes, int n_in, void* d_out, int out_size, void* d_ws, size_t ws_size, hipStream_t stream) {
  const int T = in_sizes[0] / LD, nseq = in_sizes[3] - 1;
  const int maxblk = T / 256 + nseq;
  hipFuncSetAttribute((const void*)attn_base, hipFuncAttributeMaxDynamicSharedMemorySize, (int)SHM_ATTN);
  hipLaunchKernelGGL(attn_base, dim3(maxblk * H), dim3(64 * NW), SHM_ATTN, stream, (const bf16*)d_in[0], (const bf16*)d_in[1], (const bf16*)d_in[2], (const int*)d_in[3], nseq, (bf16*)d_out, (unsigned*)d_ws);
}
```

```cpp
#include <hip/hip_runtime.h>
#include <hip/hip_bf16.h>
#include <stdint.h>
constexpr int H = 16, D = 128, LDE = H * D;
constexpr int LDS_BYTES = 98304;
constexpr int NWG = 256, GPH = NWG / H;

__device__ __forceinline__ unsigned seg4(uint64_t kb, unsigned nrk, uint64_t vb, uint64_t qp, unsigned nrq, uint64_t op, unsigned nh, unsigned nv, unsigned w, unsigned ldsb, unsigned nv0, unsigned k0, unsigned k1) {
  unsigned flag;
  asm volatile(
    "s_mov_b64 s[36:37], %1\n"
    "s_mov_b32 s38, %2\n"
    "s_mov_b32 s39, 0x20000\n"
    "s_mov_b64 s[44:45], %4\n"
    "s_mov_b32 s46, %5\n"
    "s_mov_b32 s47, 0x20000\n"
    "s_mov_b32 s60, %9\n"
    "s_mov_b32 s67, %10\n"
    "v_mbcnt_lo_u32_b32 v236, -1, 0\n"
    "v_mbcnt_hi_u32_b32 v236, -1, v236\n"
    "v_lshrrev_b32 v237, 4, v236\n"
    "v_bfe_u32 v238, v236, 2, 3\n"
    "v_lshlrev_b32 v238, 12, v238\n"
    "v_lshrrev_b32 v239, 5, v236\n"
    "v_lshl_add_u32 v238, v239, 6, v238\n"
    "v_and_b32 v240, 3, v236\n"
    "v_lshl_add_u32 v241, v240, 4, v238\n"
    "s_and_b32 s61, s60, 1\n"
    "s_mul_i32 s61, s61, 48\n"
    "v_xor_b32 v243, s61, v241\n"
    "s_lshl_b32 s61, s60, 15\n"
    "s_add_u32 s61, s61, 4096\n"
    "v_add_u32 v243, s61, v243\n"
    "v_add_u32 v240, -896, v243\n"
    "s_lshl_b32 s61, s60, 11\n"
    "s_add_u32 s61, s61, s67\n"
    "s_add_u32 m0, s61, 73728\n"
    "s_nop 0\n"
    "buffer_load_dwordx4 v243, s[36:39], 0 offen nt lds\n"
    "buffer_load_dwordx4 v240, s[36:39], 0 offen offset:1024 nt lds\n"
    "v_xor_b32 v242, 48, v241\n"
    "s_lshl_b32 s61, s60, 16\n"
    "s_add_u32 s61, s61, 4096\n"
    "v_add_u32 v228, s61, v241\n"
    "v_add_u32 v229, -896, v228\n"
    "v_add_u32 v230, s61, v242\n"
    "v_add_u32 v231, 29824, v230\n"
    "v_add_u32 v230, 30720, v230\n"
    "s_lshl_b32 s54, s60, 12\n"
    "s_add_u32 s54, s54, s67\n"
    "s_mov_b32 s52, 0x20000\n"
    "s_sub_u32 s44, s44, 4096\n"
    "s_subb_u32 s45, s45, 0\n"
    "s_add_u32 s46, s46, 4096\n"
    "s_lshl_b32 s64, s60, 14\n"
    "s_add_u32 s64, s64, 98304\n"
    "s_add_u32 s76, s64, s67\n"
    "s_mul_i32 s77, s60, 196608\n"
    "s_add_u32 m0, s76, 0\n"
    "s_nop 0\n"
    "buffer_load_dwordx4 v228, s[44:47], s77 offen offset:0 nt lds\n"
    "buffer_load_dwordx4 v230, s[44:47], s77 offen offset:2048 nt lds\n"
    "buffer_load_dwordx4 v229, s[44:47], s77 offen offset:1024 nt lds\n"
    "buffer_load_dwordx4 v231, s[44:47], s77 offen offset:3072 nt lds\n"
    "v_accvgpr_write_b32 a96, 0\n"
    "v_accvgpr_write_b32 a97, 0\n"
    "v_accvgpr_write_b32 a98, 0\n"
    "v_accvgpr_write_b32 a99, 0\n"
    "s_add_u32 m0, s54, 0\n"
    "s_nop 0\n"
    "buffer_load_dwordx4 v228, s[36:39], s52 offen offset:0 nt lds\n"
    "s_mov_b64 s[40:41], %3\n"
    "s_mov_b32 s42, %2\n"
    "v_mfma_f32_32x32x16_bf16 v[96:111], a[96:99], a[96:99], 0\n"
    "s_mov_b32 s43, 0x20000\n"
    "v_bfe_u32 v238, v236, 1, 3\n"
    "v_lshlrev_b32 v238, 12, v238\n"
    "v_lshl_add_u32 v238, v237, 5, v238\n"
    "v_and_b32 v239, 1, v236\n"
    "v_lshl_add_u32 v238, v239, 4, v238\n"
    "v_add_u32 v232, s61, v238\n"
    "v_add_u32 v233, -896, v232\n"
    "buffer_load_dwordx4 v229, s[36:39], s52 offen offset:1024 nt lds\n"
    "v_add_u32 v234, 30720, v232\n"
    "v_add_u32 v235, 29824, v232\n"
    "s_add_u32 s55, s54, 16384\n"
    "s_mov_b32 s53, 0\n"
    "v_mfma_f32_32x32x16_bf16 v[112:127], a[96:99], a[96:99], 0\n"
    "s_mov_b64 s[48:49], %6\n"
    "s_mov_b32 s50, %5\n"
    "s_mov_b32 s51, 0x20000\n"
    "s_mov_b32 s66, %7\n"
    "s_mov_b32 s65, %8\n"
    "s_mov_b32 s62, 0x3e0293ee\n"
    "buffer_load_dwordx4 v230, s[36:39], s52 offen offset:2048 nt lds\n"
    "s_mov_b32 s63, 0x3e0293ee\n"
    "v_bfe_u32 v238, v236, 3, 1\n"
    "v_mul_u32_u24 v239, 3, v238\n"
    "v_xor_b32 v239, v239, v237\n"
    "v_lshlrev_b32 v238, 11, v238\n"
    "v_lshl_add_u32 v238, v239, 4, v238\n"
    "v_mfma_f32_32x32x16_bf16 v[128:143], a[96:99], a[96:99], 0\n"
    "v_and_b32 v239, 7, v236\n"
    "v_lshl_add_u32 v238, v239, 6, v238\n"
    "v_add_u32 v226, s67, v238\n"
    "v_lshrrev_b32 v238, 5, v236\n"
    "buffer_load_dwordx4 v231, s[36:39], s52 offen offset:3072 nt lds\n"
    "v_lshlrev_b32 v238, 11, v238\n"
    "v_and_b32 v239, 31, v236\n"
    "v_lshl_add_u32 v238, v239, 3, v238\n"
    "v_add_u32 v238, 16384, v238\n"
    "v_add_u32 v227, s67, v238\n"
    "v_mov_b32 v224, v226\n"
    "v_mov_b32 v225, v227\n"
    "s_mov_b32 s58, 0\n"
    "v_mfma_f32_32x32x16_bf16 v[144:159], a[96:99], a[96:99], 0\n"
    "s_mov_b32 s59, 65536\n"
    "v_lshlrev_b32 v238, 2, v237\n"
    "s_add_u32 m0, s76, 4096\n"
    "s_add_u32 s77, s77, 65536\n"
    "buffer_load_dwordx4 v228, s[44:47], s77 offen offset:0 nt lds\n"
    "v_sub_u32 v248, s65, v238\n"
    "v_sub_u32 v250, %11, v238\n"
    "v_mov_b32 v249, 0xff800000\n"
    "v_and_b32 v238, 3, v236\n"
    "v_cmp_eq_u32 vcc, 0, v238\n"
    "v_mov_b32 v239, 0x3f803f80\n"
    "s_nop 0\n"
    "v_cndmask_b32 v238, 0, v239, vcc\n"
    "v_accvgpr_write_b32 a144, v238\n"
    "v_accvgpr_write_b32 a145, v238\n"
    "v_mfma_f32_32x32x16_bf16 v[160:175], a[96:99], a[96:99], 0\n"
    "buffer_load_dwordx4 v230, s[44:47], s77 offen offset:2048 nt lds\n"
    "v_accvgpr_write_b32 a146, v238\n"
    "v_accvgpr_write_b32 a147, v238\n"
    "v_bfe_u32 v238, v236, 1, 1\n"
    "v_lshrrev_b32 v240, 5, v236\n"
    "v_cmp_eq_u32 vcc, v238, v240\n"
    "v_accvgpr_write_b32 a152, 0\n"
    "v_accvgpr_write_b32 a153, 0\n"
    "v_accvgpr_write_b32 a154, 0\n"
    "v_accvgpr_write_b32 a155, 0\n"
    "v_accvgpr_write_b32 a156, 0\n"
    "buffer_load_dwordx4 v229, s[44:47], s77 offen offset:1024 nt lds\n"
    "v_accvgpr_write_b32 a157, 0\n"
    "v_accvgpr_write_b32 a158, 0\n"
    "v_mfma_f32_32x32x16_bf16 v[176:191], a[96:99], a[96:99], 0\n"
    "v_accvgpr_write_b32 a159, 0\n"
    "v_cndmask_b32 v238, 0, v239, vcc\n"
    "v_accvgpr_write_b32 a192, v238\n"
    "v_accvgpr_write_b32 a193, v238\n"
    "v_accvgpr_write_b32 a194, v238\n"
    "v_accvgpr_write_b32 a195, v238\n"
    "s_cmp_lt_u32 s65, 32\n"
    "s_cselect_b32 s57, 1, 0\n"
    "buffer_load_dwordx4 v231, s[44:47], s77 offen offset:3072 nt lds\n"
    "s_cmp_ge_u32 s66, 3\n"
    "s_cselect_b32 s57, s57, 0\n"
    "s_sub_u32 s56, s66, s57\n"
    "s_sub_u32 s56, s56, 2\n"
    "v_mfma_f32_32x32x16_bf16 a[128:143], a[96:99], a[96:99], 0\n"
    "s_add_u32 s61, s64, s67\n"
    "v_and_b32 v238, 15, v236\n"
    "v_mul_u32_u24 v239, 272, v238\n"
    "v_lshl_add_u32 v239, v237, 3, v239\n"
    "v_add_u32 v251, s61, v239\n"
    "v_mul_u32_u24 v239, 272, v237\n"
    "v_lshl_add_u32 v239, v238, 4, v239\n"
    "v_add_u32 v252, s61, v239\n"
    "s_mul_i32 s61, s60, 64\n"
    "v_add_u32 v239, s61, v237\n"
    "v_lshlrev_b32 v239, 12, v239\n"
    "v_lshl_add_u32 v253, v238, 4, v239\n"
    "v_add_u32 v243, s64, v226\n"
    "v_add_u32 v242, 73728, v226\n"
    "s_waitcnt vmcnt(10)\n"
    "ds_read_b128 v[192:195], v243 offset:0\n"
    "ds_read_b128 v[196:199], v243 offset:512\n"
    "s_waitcnt vmcnt(8)\n"
    "ds_read_b128 v[200:203], v243 offset:1024\n"
    "ds_read_b128 v[204:207], v243 offset:1536\n"
    "s_waitcnt lgkmcnt(2)\n"
    "v_lshlrev_b32 v88, 16, v192\n"
    "v_and_b32 v89, 0xffff0000, v192\n"
    "v_pk_mul_f32 v[88:89], v[88:89], s[62:63]\n"
    "v_cvt_pk_bf16_f32 v192, v88, v89\n"
    "v_accvgpr_write_b32 a0, v192\n"
    "v_lshlrev_b32 v90, 16, v193\n"
    "v_and_b32 v91, 0xffff0000, v193\n"
    "v_pk_mul_f32 v[90:91], v[90:91], s[62:63]\n"
    "v_cvt_pk_bf16_f32 v193, v90, v91\n"
    "v_accvgpr_write_b32 a1, v193\n"
    "s_add_u32 m0, s55, 0\n"
    "s_add_u32 s52, s52, 0x40000\n"
    "buffer_load_dwordx4 v232, s[40:43], s53 offen offset:0 nt lds\n"
    "v_lshlrev_b32 v88, 16, v194\n"
    "v_and_b32 v89, 0xffff0000, v194\n"
    "v_pk_mul_f32 v[88:89], v[88:89], s[62:63]\n"
    "v_cvt_pk_bf16_f32 v194, v88, v89\n"
    "v_accvgpr_write_b32 a2, v194\n"
    "v_lshlrev_b32 v90, 16, v195\n"
    "v_and_b32 v91, 0xffff0000, v195\n"
    "v_pk_mul_f32 v[90:91], v[90:91], s[62:63]\n"
    "v_cvt_pk_bf16_f32 v195, v90, v91\n"
    "v_accvgpr_write_b32 a3, v195\n"
    "buffer_load_dwordx4 v233, s[40:43], s53 offen offset:1024 nt lds\n"
    "v_lshlrev_b32 v88, 16, v196\n"
    "v_and_b32 v89, 0xffff0000, v196\n"
    "v_pk_mul_f32 v[88:89], v[88:89], s[62:63]\n"
    "v_cvt_pk_bf16_f32 v196, v88, v89\n"
    "v_accvgpr_write_b32 a4, v196\n"
    "v_lshlrev_b32 v90, 16, v197\n"
    "v_and_b32 v91, 0xffff0000, v197\n"
    "v_pk_mul_f32 v[90:91], v[90:91], s[62:63]\n"
    "v_cvt_pk_bf16_f32 v197, v90, v91\n"
    "v_accvgpr_write_b32 a5, v197\n"
    "buffer_load_dwordx4 v234, s[40:43], s53 offen offset:2048 nt lds\n"
    "v_lshlrev_b32 v88, 16, v198\n"
    "v_and_b32 v89, 0xffff0000, v198\n"
    "v_pk_mul_f32 v[88:89], v[88:89], s[62:63]\n"
    "v_cvt_pk_bf16_f32 v198, v88, v89\n"
    "v_accvgpr_write_b32 a6, v198\n"
    "v_lshlrev_b32 v90, 16, v199\n"
    "v_and_b32 v91, 0xffff0000, v199\n"
    "v_pk_mul_f32 v[90:91], v[90:91], s[62:63]\n"
    "v_cvt_pk_bf16_f32 v199, v90, v91\n"
    "v_accvgpr_write_b32 a7, v199\n"
    "buffer_load_dwordx4 v235, s[40:43], s53 offen offset:3072 nt lds\n"
    "s_add_u32 s53, s53, 0x40000\n"
    "s_waitcnt vmcnt(8)\n"
    "s_barrier\n"
    "ds_read_b128 a[64:67], v242 offset:0\n"
    "ds_read_b128 a[68:71], v242 offset:512\n"
    "ds_read_b128 a[72:75], v242 offset:1024\n"
    "ds_read_b128 a[76:79], v242 offset:1536\n"
    "ds_read_b128 a[80:83], v242 offset:4096\n"
    "ds_read_b128 a[84:87], v242 offset:4608\n"
    "ds_read_b128 a[88:91], v242 offset:5120\n"
    "ds_read_b128 a[92:95], v242 offset:5632\n"
    "ds_read_b128 a[160:163], v226 offset:0\n"
    "ds_read_b128 a[164:167], v226 offset:512\n"
    "ds_read_b128 a[168:171], v226 offset:1024\n"
    "ds_read_b128 a[172:175], v226 offset:1536\n"
    "ds_read_b128 a[176:179], v226 offset:4096\n"
    "ds_read_b128 a[180:183], v226 offset:4608\n"
    "ds_read_b128 a[184:187], v226 offset:5120\n"
    "ds_read_b128 a[188:191], v226 offset:5632\n"
    "v_and_b32 v242, 1, v236\n"
    "v_mul_u32_u24 v242, 0xaaaa, v242\n"
    "v_add_u32 v242, 0x4444, v242\n"
    "v_lshl_or_b32 v242, v242, 16, v242\n"
    "s_waitcnt lgkmcnt(15)\n"
    "v_lshlrev_b32 v88, 16, v200\n"
    "v_and_b32 v89, 0xffff0000, v200\n"
    "v_pk_mul_f32 v[88:89], v[88:89], s[62:63]\n"
    "v_cvt_pk_bf16_f32 v200, v88, v89\n"
    "v_accvgpr_write_b32 a8, v200\n"
    "v_lshlrev_b32 v90, 16, v201\n"
    "v_and_b32 v91, 0xffff0000, v201\n"
    "v_pk_mul_f32 v[90:91], v[90:91], s[62:63]\n"
    "v_cvt_pk_bf16_f32 v201, v90, v91\n"
    "v_accvgpr_write_b32 a9, v201\n"
    "s_add_u32 m0, s76, 8192\n"
    "s_add_u32 s77, s77, 65536\n"
    "buffer_load_dwordx4 v228, s[44:47], s77 offen offset:0 nt lds\n"
    "v_lshlrev_b32 v88, 16, v202\n"
    "v_and_b32 v89, 0xffff0000, v202\n"
    "v_pk_mul_f32 v[88:89], v[88:89], s[62:63]\n"
    "v_cvt_pk_bf16_f32 v202, v88, v89\n"
    "v_accvgpr_write_b32 a10, v202\n"
    "v_lshlrev_b32 v90, 16, v203\n"
    "v_and_b32 v91, 0xffff0000, v203\n"
    "v_pk_mul_f32 v[90:91], v[90:91], s[62:63]\n"
    "v_cvt_pk_bf16_f32 v203, v90, v91\n"
    "v_accvgpr_write_b32 a11, v203\n"
    "buffer_load_dwordx4 v230, s[44:47], s77 offen offset:2048 nt lds\n"
    "v_lshlrev_b32 v88, 16, v204\n"
    "v_and_b32 v89, 0xffff0000, v204\n"
    "v_pk_mul_f32 v[88:89], v[88:89], s[62:63]\n"
    "v_cvt_pk_bf16_f32 v204, v88, v89\n"
    "v_accvgpr_write_b32 a12, v204\n"
    "v_lshlrev_b32 v90, 16, v205\n"
    "v_and_b32 v91, 0xffff0000, v205\n"
    "v_pk_mul_f32 v[90:91], v[90:91], s[62:63]\n"
    "v_cvt_pk_bf16_f32 v205, v90, v91\n"
    "v_accvgpr_write_b32 a13, v205\n"
    "buffer_load_dwordx4 v229, s[44:47], s77 offen offset:1024 nt lds\n"
    "v_lshlrev_b32 v88, 16, v206\n"
    "v_and_b32 v89, 0xffff0000, v206\n"
    "v_pk_mul_f32 v[88:89], v[88:89], s[62:63]\n"
    "v_cvt_pk_bf16_f32 v206, v88, v89\n"
    "v_accvgpr_write_b32 a14, v206\n"
    "v_lshlrev_b32 v90, 16, v207\n"
    "v_and_b32 v91, 0xffff0000, v207\n"
    "v_pk_mul_f32 v[90:91], v[90:91], s[62:63]\n"
    "v_cvt_pk_bf16_f32 v207, v90, v91\n"
    "v_accvgpr_write_b32 a15, v207\n"
    "buffer_load_dwordx4 v231, s[44:47], s77 offen offset:3072 nt lds\n"
    "s_waitcnt lgkmcnt(8)\n"
    "v_mfma_f32_16x16x32_bf16 v[0:3], a[64:67], a[0:3], 0\n"
    "v_mfma_f32_16x16x32_bf16 v[0:3], a[68:71], a[4:7], v[0:3]\n"
    "v_mfma_f32_16x16x32_bf16 v[0:3], a[72:75], a[8:11], v[0:3]\n"
    "v_mfma_f32_16x16x32_bf16 v[0:3], a[76:79], a[12:15], v[0:3]\n"
    "v_mfma_f32_16x16x32_bf16 v[4:7], a[80:83], a[0:3], 0\n"
    "v_mfma_f32_16x16x32_bf16 v[4:7], a[84:87], a[4:7], v[4:7]\n"
    "v_mfma_f32_16x16x32_bf16 v[4:7], a[88:91], a[8:11], v[4:7]\n"
    "v_mfma_f32_16x16x32_bf16 v[4:7], a[92:95], a[12:15], v[4:7]\n"
    "s_cmp_lt_u32 %11, 32\n"
    "s_cbranch_scc1 L4_m0_0x_%=\n"
    "L4_m0_0_%=:\n"
    "s_waitcnt lgkmcnt(0)\n"
    "s_nop 4\n"
    "v_max3_f32 v244, v0, v1, v2\n"
    "v_max3_f32 v238, v3, v4, v5\n"
    "v_max3_f32 v244, v244, v6, v7\n"
    "v_max_f32 v244, v244, v238\n"
    "v_mov_b32 v238, v244\n"
    "s_nop 1\n"
    "v_permlane16_swap_b32 v244, v238\n"
    "s_nop 1\n"
    "v_max_f32 v244, v244, v238\n"
    "v_mov_b32 v238, v244\n"
    "s_nop 1\n"
    "v_permlane32_swap_b32 v244, v238\n"
    "s_nop 1\n"
    "v_max_f32 v244, v244, v238\n"
    "v_sub_f32 v64, 0, v244\n"
    "v_sub_f32 v65, 0, v244\n"
    "v_sub_f32 v66, 0, v244\n"
    "v_sub_f32 v67, 0, v244\n"
    "v_sub_f32 v68, 0, v244\n"
    "v_sub_f32 v69, 0, v244\n"
    "v_sub_f32 v70, 0, v244\n"
    "v_sub_f32 v71, 0, v244\n"
    "s_cmp_eq_u32 s66, 2\n"
    "s_cbranch_scc1 L4_m1_0x_%=\n"
    "L4_m1_0_%=:\n"
    "s_nop 1\n"
    "v_mfma_f32_16x16x32_bf16 v[32:35], a[160:163], a[0:3], v[64:67]\n"
    "v_sub_f32 v6, v6, v244\n"
    "v_sub_f32 v7, v7, v244\n"
    "v_mfma_f32_16x16x32_bf16 v[32:35], a[164:167], a[4:7], v[32:35]\n"
    "v_sub_f32 v4, v4, v244\n"
    "v_sub_f32 v5, v5, v244\n"
    "v_mfma_f32_16x16x32_bf16 v[32:35], a[168:171], a[8:11], v[32:35]\n"
    "v_exp_f32 v6, v6\n"
    "v_mfma_f32_16x16x32_bf16 v[32:35], a[172:175], a[12:15], v[32:35]\n"
    "v_exp_f32 v7, v7\n"
    "v_mfma_f32_16x16x32_bf16 v[36:39], a[176:179], a[0:3], v[68:71]\n"
    "v_exp_f32 v4, v4\n"
    "v_mfma_f32_16x16x32_bf16 v[36:39], a[180:183], a[4:7], v[36:39]\n"
    "v_exp_f32 v5, v5\n"
    "v_mfma_f32_16x16x32_bf16 v[36:39], a[184:187], a[8:11], v[36:39]\n"
    "v_cvt_pk_bf16_f32 v7, v6, v7\n"
    "v_cvt_pk_bf16_f32 v6, v4, v5\n"
    "v_mfma_f32_16x16x32_bf16 v[36:39], a[188:191], a[12:15], v[36:39]\n"
    "v_sub_f32 v2, v2, v244\n"
    "v_sub_f32 v3, v3, v244\n"
    "s_waitcnt vmcnt(10)\n"
    "ds_read_b128 v[192:195], v243 offset:4096\n"
    "ds_read_b128 v[196:199], v243 offset:4608\n"
    "s_waitcnt vmcnt(8)\n"
    "ds_read_b128 v[200:203], v243 offset:5120\n"
    "ds_read_b128 v[204:207], v243 offset:5632\n"
    "s_waitcnt lgkmcnt(2)\n"
    "v_lshlrev_b32 v88, 16, v192\n"
    "v_and_b32 v89, 0xffff0000, v192\n"
    "v_pk_mul_f32 v[88:89], v[88:89], s[62:63]\n"
    "v_cvt_pk_bf16_f32 v192, v88, v89\n"
    "v_accvgpr_write_b32 a16, v192\n"
    "v_lshlrev_b32 v90, 16, v193\n"
    "v_and_b32 v91, 0xffff0000, v193\n"
    "v_pk_mul_f32 v[90:91], v[90:91], s[62:63]\n"
    "v_cvt_pk_bf16_f32 v193, v90, v91\n"
    "v_accvgpr_write_b32 a17, v193\n"
    "s_add_u32 m0, s76, 12288\n"
    "s_add_u32 s77, s77, 65536\n"
    "buffer_load_dwordx4 v228, s[44:47], s77 offen offset:0 nt lds\n"
    "v_lshlrev_b32 v88, 16, v194\n"
    "v_and_b32 v89, 0xffff0000, v194\n"
    "v_pk_mul_f32 v[88:89], v[88:89], s[62:63]\n"
    "v_cvt_pk_bf16_f32 v194, v88, v89\n"
    "v_accvgpr_write_b32 a18, v194\n"
    "v_lshlrev_b32 v90, 16, v195\n"
    "v_and_b32 v91, 0xffff0000, v195\n"
    "v_pk_mul_f32 v[90:91], v[90:91], s[62:63]\n"
    "v_cvt_pk_bf16_f32 v195, v90, v91\n"
    "v_accvgpr_write_b32 a19, v195\n"
    "buffer_load_dwordx4 v230, s[44:47], s77 offen offset:2048 nt lds\n"
    "v_lshlrev_b32 v88, 16, v196\n"
    "v_and_b32 v89, 0xffff0000, v196\n"
    "v_pk_mul_f32 v[88:89], v[88:89], s[62:63]\n"
    "v_cvt_pk_bf16_f32 v196, v88, v89\n"
    "v_accvgpr_write_b32 a20, v196\n"
    "v_lshlrev_b32 v90, 16, v197\n"
    "v_and_b32 v91, 0xffff0000, v197\n"
    "v_pk_mul_f32 v[90:91], v[90:91], s[62:63]\n"
    "v_cvt_pk_bf16_f32 v197, v90, v91\n"
    "v_accvgpr_write_b32 a21, v197\n"
    "buffer_load_dwordx4 v229, s[44:47], s77 offen offset:1024 nt lds\n"
    "v_lshlrev_b32 v88, 16, v198\n"
    "v_and_b32 v89, 0xffff0000, v198\n"
    "v_pk_mul_f32 v[88:89], v[88:89], s[62:63]\n"
    "v_cvt_pk_bf16_f32 v198, v88, v89\n"
    "v_accvgpr_write_b32 a22, v198\n"
    "v_lshlrev_b32 v90, 16, v199\n"
    "v_and_b32 v91, 0xffff0000, v199\n"
    "v_pk_mul_f32 v[90:91], v[90:91], s[62:63]\n"
    "v_cvt_pk_bf16_f32 v199, v90, v91\n"
    "v_accvgpr_write_b32 a23, v199\n"
    "buffer_load_dwordx4 v231, s[44:47], s77 offen offset:3072 nt lds\n"
    "s_waitcnt lgkmcnt(0)\n"
    "v_lshlrev_b32 v88, 16, v200\n"
    "v_and_b32 v89, 0xffff0000, v200\n"
    "v_pk_mul_f32 v[88:89], v[88:89], s[62:63]\n"
    "v_cvt_pk_bf16_f32 v200, v88, v89\n"
    "v_accvgpr_write_b32 a24, v200\n"
    "v_lshlrev_b32 v90, 16, v201\n"
    "v_and_b32 v91, 0xffff0000, v201\n"
    "v_pk_mul_f32 v[90:91], v[90:91], s[62:63]\n"
    "v_cvt_pk_bf16_f32 v201, v90, v91\n"
    "v_accvgpr_write_b32 a25, v201\n"
    "s_add_u32 m0, s54, 32768\n"
    "s_nop 0\n"
    "buffer_load_dwordx4 v228, s[36:39], s52 offen offset:0 nt lds\n"
    "v_lshlrev_b32 v88, 16, v202\n"
    "v_and_b32 v89, 0xffff0000, v202\n"
    "v_pk_mul_f32 v[88:89], v[88:89], s[62:63]\n"
    "v_cvt_pk_bf16_f32 v202, v88, v89\n"
    "v_accvgpr_write_b32 a26, v202\n"
    "v_lshlrev_b32 v90, 16, v203\n"
    "v_and_b32 v91, 0xffff0000, v203\n"
    "v_pk_mul_f32 v[90:91], v[90:91], s[62:63]\n"
    "v_cvt_pk_bf16_f32 v203, v90, v91\n"
    "v_accvgpr_write_b32 a27, v203\n"
    "buffer_load_dwordx4 v229, s[36:39], s52 offen offset:1024 nt lds\n"
    "v_lshlrev_b32 v88, 16, v204\n"
    "v_and_b32 v89, 0xffff0000, v204\n"
    "v_pk_mul_f32 v[88:89], v[88:89], s[62:63]\n"
    "v_cvt_pk_bf16_f32 v204, v88, v89\n"
    "v_accvgpr_write_b32 a28, v204\n"
    "v_lshlrev_b32 v90, 16, v205\n"
    "v_and_b32 v91, 0xffff0000, v205\n"
    "v_pk_mul_f32 v[90:91], v[90:91], s[62:63]\n"
    "v_cvt_pk_bf16_f32 v205, v90, v91\n"
    "v_accvgpr_write_b32 a29, v205\n"
    "buffer_load_dwordx4 v230, s[36:39], s52 offen offset:2048 nt lds\n"
    "v_lshlrev_b32 v88, 16, v206\n"
    "v_and_b32 v89, 0xffff0000, v206\n"
    "v_pk_mul_f32 v[88:89], v[88:89], s[62:63]\n"
    "v_cvt_pk_bf16_f32 v206, v88, v89\n"
    "v_accvgpr_write_b32 a30, v206\n"
    "v_lshlrev_b32 v90, 16, v207\n"
    "v_and_b32 v91, 0xffff0000, v207\n"
    "v_pk_mul_f32 v[90:91], v[90:91], s[62:63]\n"
    "v_cvt_pk_bf16_f32 v207, v90, v91\n"
    "v_accvgpr_write_b32 a31, v207\n"
    "buffer_load_dwordx4 v231, s[36:39], s52 offen offset:3072 nt lds\n"
    "s_nop 1\n"
    "v_mfma_f32_16x16x32_bf16 v[8:11], a[64:67], a[16:19], 0\n"
    "v_sub_f32 v0, v0, v244\n"
    "v_sub_f32 v1, v1, v244\n"
    "v_mfma_f32_16x16x32_bf16 v[8:11], a[68:71], a[20:23], v[8:11]\n"
    "v_exp_f32 v2, v2\n"
    "v_mfma_f32_16x16x32_bf16 v[8:11], a[72:75], a[24:27], v[8:11]\n"
    "v_exp_f32 v3, v3\n"
    "v_mfma_f32_16x16x32_bf16 v[8:11], a[76:79], a[28:31], v[8:11]\n"
    "v_exp_f32 v0, v0\n"
    "v_mfma_f32_16x16x32_bf16 v[12:15], a[80:83], a[16:19], 0\n"
    "v_exp_f32 v1, v1\n"
    "v_mfma_f32_16x16x32_bf16 v[12:15], a[84:87], a[20:23], v[12:15]\n"
    "v_cvt_pk_bf16_f32 v5, v2, v3\n"
    "v_cvt_pk_bf16_f32 v4, v0, v1\n"
    "v_mfma_f32_16x16x32_bf16 v[12:15], a[88:91], a[24:27], v[12:15]\n"
    "v_mfma_f32_16x16x32_bf16 v[12:15], a[92:95], a[28:31], v[12:15]\n"
    "s_cmp_lt_u32 %11, 32\n"
    "s_cbranch_scc1 L4_m0_1x_%=\n"
    "L4_m0_1_%=:\n"
    "s_nop 5\n"
    "v_max3_f32 v245, v8, v9, v10\n"
    "v_max3_f32 v239, v11, v12, v13\n"
    "v_max3_f32 v245, v245, v14, v15\n"
    "v_max_f32 v245, v245, v239\n"
    "v_mov_b32 v239, v245\n"
    "s_nop 1\n"
    "v_permlane16_swap_b32 v245, v239\n"
    "s_nop 1\n"
    "v_max_f32 v245, v245, v239\n"
    "v_mov_b32 v239, v245\n"
    "s_nop 1\n"
    "v_permlane32_swap_b32 v245, v239\n"
    "s_nop 1\n"
    "v_max_f32 v245, v245, v239\n"
    "v_sub_f32 v72, 0, v245\n"
    "v_sub_f32 v73, 0, v245\n"
    "v_sub_f32 v74, 0, v245\n"
    "v_sub_f32 v75, 0, v245\n"
    "v_sub_f32 v76, 0, v245\n"
    "v_sub_f32 v77, 0, v245\n"
    "v_sub_f32 v78, 0, v245\n"
    "v_sub_f32 v79, 0, v245\n"
    "s_cmp_eq_u32 s66, 2\n"
    "s_cbranch_scc1 L4_m1_1x_%=\n"
    "L4_m1_1_%=:\n"
    "s_nop 1\n"
    "v_mfma_f32_16x16x32_bf16 v[40:43], a[160:163], a[16:19], v[72:75]\n"
    "v_sub_f32 v8, v8, v245\n"
    "v_sub_f32 v9, v9, v245\n"
    "v_mfma_f32_16x16x32_bf16 v[40:43], a[164:167], a[20:23], v[40:43]\n"
    "v_sub_f32 v10, v10, v245\n"
    "v_sub_f32 v11, v11, v245\n"
    "v_mfma_f32_16x16x32_bf16 v[40:43], a[168:171], a[24:27], v[40:43]\n"
    "v_exp_f32 v8, v8\n"
    "v_mfma_f32_16x16x32_bf16 v[40:43], a[172:175], a[28:31], v[40:43]\n"
    "v_exp_f32 v9, v9\n"
    "v_mfma_f32_16x16x32_bf16 v[44:47], a[176:179], a[16:19], v[76:79]\n"
    "v_exp_f32 v10, v10\n"
    "v_mfma_f32_16x16x32_bf16 v[44:47], a[180:183], a[20:23], v[44:47]\n"
    "v_exp_f32 v11, v11\n"
    "v_mfma_f32_16x16x32_bf16 v[44:47], a[184:187], a[24:27], v[44:47]\n"
    "v_cvt_pk_bf16_f32 v8, v8, v9\n"
    "v_cvt_pk_bf16_f32 v9, v10, v11\n"
    "v_mfma_f32_16x16x32_bf16 v[44:47], a[188:191], a[28:31], v[44:47]\n"
    "v_sub_f32 v12, v12, v245\n"
    "v_sub_f32 v13, v13, v245\n"
    "s_waitcnt vmcnt(10)\n"
    "ds_read_b128 v[192:195], v243 offset:8192\n"
    "ds_read_b128 v[196:199], v243 offset:8704\n"
    "s_waitcnt vmcnt(8)\n"
    "ds_read_b128 v[200:203], v243 offset:9216\n"
    "ds_read_b128 v[204:207], v243 offset:9728\n"
    "s_waitcnt lgkmcnt(2)\n"
    "v_lshlrev_b32 v88, 16, v192\n"
    "v_and_b32 v89, 0xffff0000, v192\n"
    "v_pk_mul_f32 v[88:89], v[88:89], s[62:63]\n"
    "v_cvt_pk_bf16_f32 v192, v88, v89\n"
    "v_accvgpr_write_b32 a32, v192\n"
    "v_lshlrev_b32 v90, 16, v193\n"
    "v_and_b32 v91, 0xffff0000, v193\n"
    "v_pk_mul_f32 v[90:91], v[90:91], s[62:63]\n"
    "v_cvt_pk_bf16_f32 v193, v90, v91\n"
    "v_accvgpr_write_b32 a33, v193\n"
    "s_add_u32 m0, s55, 32768\n"
    "s_add_u32 s52, s52, 0x40000\n"
    "buffer_load_dwordx4 v232, s[40:43], s53 offen offset:0 nt lds\n"
    "v_lshlrev_b32 v88, 16, v194\n"
    "v_and_b32 v89, 0xffff0000, v194\n"
    "v_pk_mul_f32 v[88:89], v[88:89], s[62:63]\n"
    "v_cvt_pk_bf16_f32 v194, v88, v89\n"
    "v_accvgpr_write_b32 a34, v194\n"
    "v_lshlrev_b32 v90, 16, v195\n"
    "v_and_b32 v91, 0xffff0000, v195\n"
    "v_pk_mul_f32 v[90:91], v[90:91], s[62:63]\n"
    "v_cvt_pk_bf16_f32 v195, v90, v91\n"
    "v_accvgpr_write_b32 a35, v195\n"
    "buffer_load_dwordx4 v233, s[40:43], s53 offen offset:1024 nt lds\n"
    "v_lshlrev_b32 v88, 16, v196\n"
    "v_and_b32 v89, 0xffff0000, v196\n"
    "v_pk_mul_f32 v[88:89], v[88:89], s[62:63]\n"
    "v_cvt_pk_bf16_f32 v196, v88, v89\n"
    "v_accvgpr_write_b32 a36, v196\n"
    "v_lshlrev_b32 v90, 16, v197\n"
    "v_and_b32 v91, 0xffff0000, v197\n"
    "v_pk_mul_f32 v[90:91], v[90:91], s[62:63]\n"
    "v_cvt_pk_bf16_f32 v197, v90, v91\n"
    "v_accvgpr_write_b32 a37, v197\n"
    "buffer_load_dwordx4 v234, s[40:43], s53 offen offset:2048 nt lds\n"
    "v_lshlrev_b32 v88, 16, v198\n"
    "v_and_b32 v89, 0xffff0000, v198\n"
    "v_pk_mul_f32 v[88:89], v[88:89], s[62:63]\n"
    "v_cvt_pk_bf16_f32 v198, v88, v89\n"
    "v_accvgpr_write_b32 a38, v198\n"
    "v_lshlrev_b32 v90, 16, v199\n"
    "v_and_b32 v91, 0xffff0000, v199\n"
    "v_pk_mul_f32 v[90:91], v[90:91], s[62:63]\n"
    "v_cvt_pk_bf16_f32 v199, v90, v91\n"
    "v_accvgpr_write_b32 a39, v199\n"
    "buffer_load_dwordx4 v235, s[40:43], s53 offen offset:3072 nt lds\n"
    "s_add_u32 s53, s53, 0x40000\n"
    "s_waitcnt lgkmcnt(0)\n"
    "v_lshlrev_b32 v88, 16, v200\n"
    "v_and_b32 v89, 0xffff0000, v200\n"
    "v_pk_mul_f32 v[88:89], v[88:89], s[62:63]\n"
    "v_cvt_pk_bf16_f32 v200, v88, v89\n"
    "v_accvgpr_write_b32 a40, v200\n"
    "v_lshlrev_b32 v90, 16, v201\n"
    "v_and_b32 v91, 0xffff0000, v201\n"
    "v_pk_mul_f32 v[90:91], v[90:91], s[62:63]\n"
    "v_cvt_pk_bf16_f32 v201, v90, v91\n"
    "v_accvgpr_write_b32 a41, v201\n"
    "v_lshlrev_b32 v88, 16, v202\n"
    "v_and_b32 v89, 0xffff0000, v202\n"
    "v_pk_mul_f32 v[88:89], v[88:89], s[62:63]\n"
    "v_cvt_pk_bf16_f32 v202, v88, v89\n"
    "v_accvgpr_write_b32 a42, v202\n"
    "v_lshlrev_b32 v90, 16, v203\n"
    "v_and_b32 v91, 0xffff0000, v203\n"
    "v_pk_mul_f32 v[90:91], v[90:91], s[62:63]\n"
    "v_cvt_pk_bf16_f32 v203, v90, v91\n"
    "v_accvgpr_write_b32 a43, v203\n"
    "v_lshlrev_b32 v88, 16, v204\n"
    "v_and_b32 v89, 0xffff0000, v204\n"
    "v_pk_mul_f32 v[88:89], v[88:89], s[62:63]\n"
    "v_cvt_pk_bf16_f32 v204, v88, v89\n"
    "v_accvgpr_write_b32 a44, v204\n"
    "v_lshlrev_b32 v90, 16, v205\n"
    "v_and_b32 v91, 0xffff0000, v205\n"
    "v_pk_mul_f32 v[90:91], v[90:91], s[62:63]\n"
    "v_cvt_pk_bf16_f32 v205, v90, v91\n"
    "v_accvgpr_write_b32 a45, v205\n"
    "v_lshlrev_b32 v88, 16, v206\n"
    "v_and_b32 v89, 0xffff0000, v206\n"
    "v_pk_mul_f32 v[88:89], v[88:89], s[62:63]\n"
    "v_cvt_pk_bf16_f32 v206, v88, v89\n"
    "v_accvgpr_write_b32 a46, v206\n"
    "v_lshlrev_b32 v90, 16, v207\n"
    "v_and_b32 v91, 0xffff0000, v207\n"
    "v_pk_mul_f32 v[90:91], v[90:91], s[62:63]\n"
    "v_cvt_pk_bf16_f32 v207, v90, v91\n"
    "v_accvgpr_write_b32 a47, v207\n"
    "s_nop 1\n"
    "v_mfma_f32_16x16x32_bf16 v[16:19], a[64:67], a[32:35], 0\n"
    "v_sub_f32 v14, v14, v245\n"
    "v_sub_f32 v15, v15, v245\n"
    "v_mfma_f32_16x16x32_bf16 v[16:19], a[68:71], a[36:39], v[16:19]\n"
    "v_exp_f32 v12, v12\n"
    "v_mfma_f32_16x16x32_bf16 v[16:19], a[72:75], a[40:43], v[16:19]\n"
    "v_exp_f32 v13, v13\n"
    "v_mfma_f32_16x16x32_bf16 v[16:19], a[76:79], a[44:47], v[16:19]\n"
    "v_exp_f32 v14, v14\n"
    "v_mfma_f32_16x16x32_bf16 v[20:23], a[80:83], a[32:35], 0\n"
    "v_exp_f32 v15, v15\n"
    "v_mfma_f32_16x16x32_bf16 v[20:23], a[84:87], a[36:39], v[20:23]\n"
    "v_cvt_pk_bf16_f32 v10, v12, v13\n"
    "v_cvt_pk_bf16_f32 v11, v14, v15\n"
    "v_mfma_f32_16x16x32_bf16 v[20:23], a[88:91], a[40:43], v[20:23]\n"
    "v_mfma_f32_16x16x32_bf16 v[20:23], a[92:95], a[44:47], v[20:23]\n"
    "s_cmp_lt_u32 %11, 32\n"
    "s_cbranch_scc1 L4_m0_2x_%=\n"
    "L4_m0_2_%=:\n"
    "s_nop 5\n"
    "v_max3_f32 v246, v16, v17, v18\n"
    "v_max3_f32 v240, v19, v20, v21\n"
    "v_max3_f32 v246, v246, v22, v23\n"
    "v_max_f32 v246, v246, v240\n"
    "v_mov_b32 v240, v246\n"
    "s_nop 1\n"
    "v_permlane16_swap_b32 v246, v240\n"
    "s_nop 1\n"
    "v_max_f32 v246, v246, v240\n"
    "v_mov_b32 v240, v246\n"
    "s_nop 1\n"
    "v_permlane32_swap_b32 v246, v240\n"
    "s_nop 1\n"
    "v_max_f32 v246, v246, v240\n"
    "v_sub_f32 v80, 0, v246\n"
    "v_sub_f32 v81, 0, v246\n"
    "v_sub_f32 v82, 0, v246\n"
    "v_sub_f32 v83, 0, v246\n"
    "v_sub_f32 v84, 0, v246\n"
    "v_sub_f32 v85, 0, v246\n"
    "v_sub_f32 v86, 0, v246\n"
    "v_sub_f32 v87, 0, v246\n"
    "s_cmp_eq_u32 s66, 2\n"
    "s_cbranch_scc1 L4_m1_2x_%=\n"
    "L4_m1_2_%=:\n"
    "s_nop 1\n"
    "v_mfma_f32_16x16x32_bf16 v[48:51], a[160:163], a[32:35], v[80:83]\n"
    "v_sub_f32 v22, v22, v246\n"
    "v_sub_f32 v23, v23, v246\n"
    "v_mfma_f32_16x16x32_bf16 v[48:51], a[164:167], a[36:39], v[48:51]\n"
    "v_sub_f32 v20, v20, v246\n"
    "v_sub_f32 v21, v21, v246\n"
    "v_mfma_f32_16x16x32_bf16 v[48:51], a[168:171], a[40:43], v[48:51]\n"
    "v_exp_f32 v22, v22\n"
    "v_mfma_f32_16x16x32_bf16 v[48:51], a[172:175], a[44:47], v[48:51]\n"
    "v_exp_f32 v23, v23\n"
    "v_mfma_f32_16x16x32_bf16 v[52:55], a[176:179], a[32:35], v[84:87]\n"
    "v_exp_f32 v20, v20\n"
    "v_mfma_f32_16x16x32_bf16 v[52:55], a[180:183], a[36:39], v[52:55]\n"
    "v_exp_f32 v21, v21\n"
    "v_mfma_f32_16x16x32_bf16 v[52:55], a[184:187], a[40:43], v[52:55]\n"
    "v_cvt_pk_bf16_f32 v23, v22, v23\n"
    "v_cvt_pk_bf16_f32 v22, v20, v21\n"
    "v_mfma_f32_16x16x32_bf16 v[52:55], a[188:191], a[44:47], v[52:55]\n"
    "v_sub_f32 v18, v18, v246\n"
    "v_sub_f32 v19, v19, v246\n"
    "s_waitcnt vmcnt(16)\n"
    "s_barrier\n"
    "ds_read_b64_tr_b16 a[100:101], v225 offset:256\n"
    "ds_read_b64_tr_b16 a[102:103], v225 offset:4352\n"
    "ds_read_b64_tr_b16 a[104:105], v225 offset:512\n"
    "ds_read_b64_tr_b16 a[106:107], v225 offset:4608\n"
    "ds_read_b64_tr_b16 a[108:109], v225 offset:768\n"
    "ds_read_b64_tr_b16 a[110:111], v225 offset:4864\n"
    "ds_read_b64_tr_b16 a[112:113], v225 offset:1024\n"
    "ds_read_b64_tr_b16 a[114:115], v225 offset:5120\n"
    "ds_read_b64_tr_b16 a[116:117], v225 offset:1280\n"
    "ds_read_b64_tr_b16 a[118:119], v225 offset:5376\n"
    "ds_read_b64_tr_b16 a[120:121], v225 offset:1536\n"
    "ds_read_b64_tr_b16 a[122:123], v225 offset:5632\n"
    "ds_read_b64_tr_b16 a[124:125], v225 offset:1792\n"
    "ds_read_b64_tr_b16 a[126:127], v225 offset:5888\n"
    "s_waitcnt vmcnt(10)\n"
    "ds_read_b128 v[192:195], v243 offset:12288\n"
    "ds_read_b128 v[196:199], v243 offset:12800\n"
    "s_waitcnt vmcnt(8)\n"
    "ds_read_b128 v[200:203], v243 offset:13312\n"
    "ds_read_b128 v[204:207], v243 offset:13824\n"
    "s_waitcnt lgkmcnt(2)\n"
    "v_lshlrev_b32 v88, 16, v192\n"
    "v_and_b32 v89, 0xffff0000, v192\n"
    "v_pk_mul_f32 v[88:89], v[88:89], s[62:63]\n"
    "v_cvt_pk_bf16_f32 v192, v88, v89\n"
    "v_accvgpr_write_b32 a48, v192\n"
    "v_lshlrev_b32 v90, 16, v193\n"
    "v_and_b32 v91, 0xffff0000, v193\n"
    "v_pk_mul_f32 v[90:91], v[90:91], s[62:63]\n"
    "v_cvt_pk_bf16_f32 v193, v90, v91\n"
    "v_accvgpr_write_b32 a49, v193\n"
    "v_lshlrev_b32 v88, 16, v194\n"
    "v_and_b32 v89, 0xffff0000, v194\n"
    "v_pk_mul_f32 v[88:89], v[88:89], s[62:63]\n"
    "v_cvt_pk_bf16_f32 v194, v88, v89\n"
    "v_accvgpr_write_b32 a50, v194\n"
    "v_lshlrev_b32 v90, 16, v195\n"
    "v_and_b32 v91, 0xffff0000, v195\n"
    "v_pk_mul_f32 v[90:91], v[90:91], s[62:63]\n"
    "v_cvt_pk_bf16_f32 v195, v90, v91\n"
    "v_accvgpr_write_b32 a51, v195\n"
    "v_lshlrev_b32 v88, 16, v196\n"
    "v_and_b32 v89, 0xffff0000, v196\n"
    "v_pk_mul_f32 v[88:89], v[88:89], s[62:63]\n"
    "v_cvt_pk_bf16_f32 v196, v88, v89\n"
    "v_accvgpr_write_b32 a52, v196\n"
    "v_lshlrev_b32 v90, 16, v197\n"
    "v_and_b32 v91, 0xffff0000, v197\n"
    "v_pk_mul_f32 v[90:91], v[90:91], s[62:63]\n"
    "v_cvt_pk_bf16_f32 v197, v90, v91\n"
    "v_accvgpr_write_b32 a53, v197\n"
    "v_lshlrev_b32 v88, 16, v198\n"
    "v_and_b32 v89, 0xffff0000, v198\n"
    "v_pk_mul_f32 v[88:89], v[88:89], s[62:63]\n"
    "v_cvt_pk_bf16_f32 v198, v88, v89\n"
    "v_accvgpr_write_b32 a54, v198\n"
    "v_lshlrev_b32 v90, 16, v199\n"
    "v_and_b32 v91, 0xffff0000, v199\n"
    "v_pk_mul_f32 v[90:91], v[90:91], s[62:63]\n"
    "v_cvt_pk_bf16_f32 v199, v90, v91\n"
    "v_accvgpr_write_b32 a55, v199\n"
    "s_waitcnt lgkmcnt(0)\n"
    "v_lshlrev_b32 v88, 16, v200\n"
    "v_and_b32 v89, 0xffff0000, v200\n"
    "v_pk_mul_f32 v[88:89], v[88:89], s[62:63]\n"
    "v_cvt_pk_bf16_f32 v200, v88, v89\n"
    "v_accvgpr_write_b32 a56, v200\n"
    "v_lshlrev_b32 v90, 16, v201\n"
    "v_and_b32 v91, 0xffff0000, v201\n"
    "v_pk_mul_f32 v[90:91], v[90:91], s[62:63]\n"
    "v_cvt_pk_bf16_f32 v201, v90, v91\n"
    "v_accvgpr_write_b32 a57, v201\n"
    "v_lshlrev_b32 v88, 16, v202\n"
    "v_and_b32 v89, 0xffff0000, v202\n"
    "v_pk_mul_f32 v[88:89], v[88:89], s[62:63]\n"
    "v_cvt_pk_bf16_f32 v202, v88, v89\n"
    "v_accvgpr_write_b32 a58, v202\n"
    "v_lshlrev_b32 v90, 16, v203\n"
    "v_and_b32 v91, 0xffff0000, v203\n"
    "v_pk_mul_f32 v[90:91], v[90:91], s[62:63]\n"
    "v_cvt_pk_bf16_f32 v203, v90, v91\n"
    "v_accvgpr_write_b32 a59, v203\n"
    "v_lshlrev_b32 v88, 16, v204\n"
    "v_and_b32 v89, 0xffff0000, v204\n"
    "v_pk_mul_f32 v[88:89], v[88:89], s[62:63]\n"
    "v_cvt_pk_bf16_f32 v204, v88, v89\n"
    "v_accvgpr_write_b32 a60, v204\n"
    "v_lshlrev_b32 v90, 16, v205\n"
    "v_and_b32 v91, 0xffff0000, v205\n"
    "v_pk_mul_f32 v[90:91], v[90:91], s[62:63]\n"
    "v_cvt_pk_bf16_f32 v205, v90, v91\n"
    "v_accvgpr_write_b32 a61, v205\n"
    "v_lshlrev_b32 v88, 16, v206\n"
    "v_and_b32 v89, 0xffff0000, v206\n"
    "v_pk_mul_f32 v[88:89], v[88:89], s[62:63]\n"
    "v_cvt_pk_bf16_f32 v206, v88, v89\n"
    "v_accvgpr_write_b32 a62, v206\n"
    "v_lshlrev_b32 v90, 16, v207\n"
    "v_and_b32 v91, 0xffff0000, v207\n"
    "v_pk_mul_f32 v[90:91], v[90:91], s[62:63]\n"
    "v_cvt_pk_bf16_f32 v207, v90, v91\n"
    "v_accvgpr_write_b32 a63, v207\n"
    "s_nop 1\n"
    "v_mfma_f32_16x16x32_bf16 v[24:27], a[64:67], a[48:51], 0\n"
    "v_sub_f32 v16, v16, v246\n"
    "v_sub_f32 v17, v17, v246\n"
    "v_mfma_f32_16x16x32_bf16 v[24:27], a[68:71], a[52:55], v[24:27]\n"
    "v_exp_f32 v18, v18\n"
    "v_mfma_f32_16x16x32_bf16 v[24:27], a[72:75], a[56:59], v[24:27]\n"
    "v_exp_f32 v19, v19\n"
    "v_mfma_f32_16x16x32_bf16 v[24:27], a[76:79], a[60:63], v[24:27]\n"
    "v_exp_f32 v16, v16\n"
    "v_mfma_f32_16x16x32_bf16 v[28:31], a[80:83], a[48:51], 0\n"
    "v_exp_f32 v17, v17\n"
    "v_mfma_f32_16x16x32_bf16 v[28:31], a[84:87], a[52:55], v[28:31]\n"
    "v_cvt_pk_bf16_f32 v21, v18, v19\n"
    "v_cvt_pk_bf16_f32 v20, v16, v17\n"
    "v_mfma_f32_16x16x32_bf16 v[28:31], a[88:91], a[56:59], v[28:31]\n"
    "v_mfma_f32_16x16x32_bf16 v[28:31], a[92:95], a[60:63], v[28:31]\n"
    "v_mfma_f32_32x32x16_bf16 v[192:207], a[96:99], a[96:99], 0\n"
    "v_mfma_f32_32x32x16_bf16 v[208:223], a[96:99], a[96:99], 0\n"
    "s_cmp_lt_u32 %11, 32\n"
    "s_cbranch_scc1 L4_m0_3x_%=\n"
    "L4_m0_3_%=:\n"
    "s_nop 3\n"
    "v_max3_f32 v247, v24, v25, v26\n"
    "v_max3_f32 v241, v27, v28, v29\n"
    "v_max3_f32 v247, v247, v30, v31\n"
    "v_max_f32 v247, v247, v241\n"
    "v_mov_b32 v241, v247\n"
    "s_nop 1\n"
    "v_permlane16_swap_b32 v247, v241\n"
    "s_nop 1\n"
    "v_max_f32 v247, v247, v241\n"
    "v_mov_b32 v241, v247\n"
    "s_nop 1\n"
    "v_permlane32_swap_b32 v247, v241\n"
    "s_nop 1\n"
    "v_max_f32 v247, v247, v241\n"
    "v_sub_f32 v88, 0, v247\n"
    "v_sub_f32 v89, 0, v247\n"
    "v_sub_f32 v90, 0, v247\n"
    "v_sub_f32 v91, 0, v247\n"
    "v_sub_f32 v92, 0, v247\n"
    "v_sub_f32 v93, 0, v247\n"
    "v_sub_f32 v94, 0, v247\n"
    "v_sub_f32 v95, 0, v247\n"
    "s_cmp_eq_u32 s66, 2\n"
    "s_cbranch_scc1 L4_m1_3x_%=\n"
    "L4_m1_3_%=:\n"
    "s_nop 1\n"
    "v_mfma_f32_16x16x32_bf16 v[56:59], a[160:163], a[48:51], v[88:91]\n"
    "v_sub_f32 v24, v24, v247\n"
    "v_sub_f32 v25, v25, v247\n"
    "v_mfma_f32_16x16x32_bf16 v[56:59], a[164:167], a[52:55], v[56:59]\n"
    "v_sub_f32 v26, v26, v247\n"
    "v_sub_f32 v27, v27, v247\n"
    "v_mfma_f32_16x16x32_bf16 v[56:59], a[168:171], a[56:59], v[56:59]\n"
    "v_exp_f32 v24, v24\n"
    "v_mfma_f32_16x16x32_bf16 v[56:59], a[172:175], a[60:63], v[56:59]\n"
    "v_exp_f32 v25, v25\n"
    "v_mfma_f32_16x16x32_bf16 v[60:63], a[176:179], a[48:51], v[92:95]\n"
    "v_exp_f32 v26, v26\n"
    "v_mfma_f32_16x16x32_bf16 v[60:63], a[180:183], a[52:55], v[60:63]\n"
    "v_exp_f32 v27, v27\n"
    "v_mfma_f32_16x16x32_bf16 v[60:63], a[184:187], a[56:59], v[60:63]\n"
    "v_cvt_pk_bf16_f32 v24, v24, v25\n"
    "v_cvt_pk_bf16_f32 v25, v26, v27\n"
    "v_mfma_f32_16x16x32_bf16 v[60:63], a[188:191], a[60:63], v[60:63]\n"
    "v_sub_f32 v28, v28, v247\n"
    "v_sub_f32 v29, v29, v247\n"
    "ds_read_b64_tr_b16 a[96:97], v225 offset:0\n"
    "ds_read_b64_tr_b16 a[98:99], v225 offset:4096\n"
    "s_add_u32 m0, s54, s59\n"
    "v_sub_f32 v30, v30, v247\n"
    "v_sub_f32 v31, v31, v247\n"
    "v_exp_f32 v28, v28\n"
    "buffer_load_dwordx4 v228, s[36:39], s52 offen offset:0 nt lds\n"
    "v_exp_f32 v29, v29\n"
    "v_exp_f32 v30, v30\n"
    "v_exp_f32 v31, v31\n"
    "buffer_load_dwordx4 v229, s[36:39], s52 offen offset:1024 nt lds\n"
    "v_cvt_pk_bf16_f32 v26, v28, v29\n"
    "v_cvt_pk_bf16_f32 v27, v30, v31\n"
    "buffer_load_dwordx4 v230, s[36:39], s52 offen offset:2048 nt lds\n"
    "buffer_load_dwordx4 v231, s[36:39], s52 offen offset:3072 nt lds\n"
    "s_add_u32 s52, s52, 0x40000\n"
    "s_waitcnt lgkmcnt(0)\n"
    "s_sub_u32 s56, s56, 1\n"
    "s_cbranch_scc1 L4_ev0_%=\n"
    "s_branch L4_p1_%=\n"
    ".p2align 6\n"
    "L4_p0_%=:\n"
    "v_mfma_f32_16x16x32_bf16 v[96:99], a[96:99], v[36:39], v[96:99]\n"
    "s_waitcnt vmcnt(8)\n"
    "s_barrier\n"
    "v_mfma_f32_16x16x32_bf16 v[100:103], a[100:103], v[36:39], v[100:103]\n"
    "s_add_u32 m0, s54, s59\n"
    "v_exp_f32 v6, v6\n"
    "v_mfma_f32_16x16x32_bf16 v[104:107], a[104:107], v[36:39], v[104:107]\n"
    "v_exp_f32 v7, v7\n"
    "v_mfma_f32_16x16x32_bf16 v[108:111], a[108:111], v[36:39], v[108:111]\n"
    "ds_read_b128 a[64:67], v224 offset:0\n"
    "v_cvt_pk_bf16_f32 v7, v6, v7\n"
    "v_mfma_f32_16x16x32_bf16 v[112:115], a[112:115], v[36:39], v[112:115]\n"
    "v_exp_f32 v4, v4\n"
    "v_mfma_f32_16x16x32_bf16 v[116:119], a[116:119], v[36:39], v[116:119]\n"
    "ds_read_b128 a[68:71], v224 offset:512\n"
    "v_mfma_f32_16x16x32_bf16 v[120:123], a[120:123], v[36:39], v[120:123]\n"
    "v_exp_f32 v5, v5\n"
    "v_mfma_f32_16x16x32_bf16 v[124:127], a[124:127], v[36:39], v[124:127]\n"
    "ds_read_b128 a[72:75], v224 offset:1024\n"
    "v_cvt_pk_bf16_f32 v6, v4, v5\n"
    "v_mfma_f32_16x16x32_bf16 v[128:131], a[96:99], v[40:43], v[128:131]\n"
    "v_exp_f32 v2, v2\n"
    "v_mfma_f32_16x16x32_bf16 v[132:135], a[100:103], v[40:43], v[132:135]\n"
    "buffer_load_dwordx4 v228, s[36:39], s52 offen offset:0 lds\n"
    "v_mfma_f32_16x16x32_bf16 v[136:139], a[104:107], v[40:43], v[136:139]\n"
    "v_exp_f32 v3, v3\n"
    "v_mfma_f32_16x16x32_bf16 v[140:143], a[108:111], v[40:43], v[140:143]\n"
    "ds_read_b128 a[76:79], v224 offset:1536\n"
    "v_cvt_pk_bf16_f32 v5, v2, v3\n"
    "v_mfma_f32_16x16x32_bf16 v[144:147], a[112:115], v[40:43], v[144:147]\n"
    "v_exp_f32 v0, v0\n"
    "v_mfma_f32_16x16x32_bf16 v[148:151], a[116:119], v[40:43], v[148:151]\n"
    "ds_read_b128 a[80:83], v224 offset:4096\n"
    "v_mfma_f32_16x16x32_bf16 v[152:155], a[120:123], v[40:43], v[152:155]\n"
    "v_exp_f32 v1, v1\n"
    "v_mfma_f32_16x16x32_bf16 v[156:159], a[124:127], v[40:43], v[156:159]\n"
    "v_cvt_pk_bf16_f32 v4, v0, v1\n"
    "v_smfmac_f32_16x16x64_bf16 a[152:155], a[192:195], v[36:43], v242\n"
    "v_exp_f32 v8, v8\n"
    "v_mfma_f32_16x16x32_bf16 v[160:163], a[96:99], v[52:55], v[160:163]\n"
    "buffer_load_dwordx4 v229, s[36:39], s52 offen offset:1024 lds\n"
    "v_mfma_f32_16x16x32_bf16 v[164:167], a[100:103], v[52:55], v[164:167]\n"
    "v_exp_f32 v9, v9\n"
    "v_mfma_f32_16x16x32_bf16 v[168:171], a[104:107], v[52:55], v[168:171]\n"
    "ds_read_b128 a[84:87], v224 offset:4608\n"
    "v_cvt_pk_bf16_f32 v8, v8, v9\n"
    "v_mfma_f32_16x16x32_bf16 v[172:175], a[108:111], v[52:55], v[172:175]\n"
    "v_exp_f32 v10, v10\n"
    "v_mfma_f32_16x16x32_bf16 v[176:179], a[112:115], v[52:55], v[176:179]\n"
    "ds_read_b128 a[88:91], v224 offset:5120\n"
    "v_mfma_f32_16x16x32_bf16 v[180:183], a[116:119], v[52:55], v[180:183]\n"
    "v_exp_f32 v11, v11\n"
    "v_mfma_f32_16x16x32_bf16 v[184:187], a[120:123], v[52:55], v[184:187]\n"
    "buffer_load_dwordx4 v230, s[36:39], s52 offen offset:2048 lds\n"
    "v_mfma_f32_16x16x32_bf16 v[188:191], a[124:127], v[52:55], v[188:191]\n"
    "v_exp_f32 v12, v12\n"
    "v_mfma_f32_16x16x32_bf16 v[192:195], a[96:99], v[56:59], v[192:195]\n"
    "ds_read_b128 a[92:95], v224 offset:5632\n"
    "v_cvt_pk_bf16_f32 v9, v10, v11\n"
    "v_mfma_f32_16x16x32_bf16 v[196:199], a[100:103], v[56:59], v[196:199]\n"
    "v_exp_f32 v13, v13\n"
    "v_mfma_f32_16x16x32_bf16 v[200:203], a[104:107], v[56:59], v[200:203]\n"
    "ds_read_b64_tr_b16 a[96:97], v225 offset:0\n"
    "v_cvt_pk_bf16_f32 v10, v12, v13\n"
    "v_mfma_f32_16x16x32_bf16 v[204:207], a[108:111], v[56:59], v[204:207]\n"
    "v_exp_f32 v14, v14\n"
    "v_mfma_f32_16x16x32_bf16 v[208:211], a[112:115], v[56:59], v[208:211]\n"
    "buffer_load_dwordx4 v231, s[36:39], s52 offen offset:3072 lds\n"
    "v_mfma_f32_16x16x32_bf16 v[212:215], a[116:119], v[56:59], v[212:215]\n"
    "v_exp_f32 v15, v15\n"
    "v_mfma_f32_16x16x32_bf16 v[216:219], a[120:123], v[56:59], v[216:219]\n"
    "ds_read_b64_tr_b16 a[98:99], v225 offset:4096\n"
    "v_cvt_pk_bf16_f32 v11, v14, v15\n"
    "v_mfma_f32_16x16x32_bf16 v[220:223], a[124:127], v[56:59], v[220:223]\n"
    "ds_read_b64_tr_b16 a[100:101], v225 offset:256\n"
    "ds_read_b64_tr_b16 a[102:103], v225 offset:4352\n"
    "v_smfmac_f32_16x16x64_bf16 a[156:159], a[192:195], v[52:59], v242\n"
    "s_waitcnt lgkmcnt(4)\n"
    "v_exp_f32 v22, v22\n"
    "v_mfma_f32_16x16x32_bf16 v[32:35], a[64:67], a[0:3], v[64:67]\n"
    "s_add_u32 s52, s52, 0x40000\n"
    "ds_read_b64_tr_b16 a[104:105], v225 offset:512\n"
    "v_mfma_f32_16x16x32_bf16 v[32:35], a[68:71], a[4:7], v[32:35]\n"
    "v_exp_f32 v23, v23\n"
    "v_mfma_f32_16x16x32_bf16 v[32:35], a[72:75], a[8:11], v[32:35]\n"
    "ds_read_b64_tr_b16 a[106:107], v225 offset:4608\n"
    "v_cvt_pk_bf16_f32 v23, v22, v23\n"
    "v_mfma_f32_16x16x32_bf16 v[32:35], a[76:79], a[12:15], v[32:35]\n"
    "v_exp_f32 v20, v20\n"
    "v_mfma_f32_16x16x32_bf16 v[36:39], a[80:83], a[0:3], v[68:71]\n"
    "ds_read_b64_tr_b16 a[108:109], v225 offset:768\n"
    "v_mfma_f32_16x16x32_bf16 v[36:39], a[84:87], a[4:7], v[36:39]\n"
    "v_exp_f32 v21, v21\n"
    "v_mfma_f32_16x16x32_bf16 v[36:39], a[88:91], a[8:11], v[36:39]\n"
    "ds_read_b64_tr_b16 a[110:111], v225 offset:4864\n"
    "v_cvt_pk_bf16_f32 v22, v20, v21\n"
    "v_mfma_f32_16x16x32_bf16 v[36:39], a[92:95], a[12:15], v[36:39]\n"
    "v_exp_f32 v18, v18\n"
    "v_mfma_f32_16x16x32_bf16 v[40:43], a[64:67], a[16:19], v[72:75]\n"
    "v_mfma_f32_16x16x32_bf16 v[40:43], a[68:71], a[20:23], v[40:43]\n"
    "v_exp_f32 v19, v19\n"
    "v_mfma_f32_16x16x32_bf16 v[40:43], a[72:75], a[24:27], v[40:43]\n"
    "ds_read_b64_tr_b16 a[112:113], v225 offset:1024\n"
    "v_cvt_pk_bf16_f32 v21, v18, v19\n"
    "v_mfma_f32_16x16x32_bf16 v[40:43], a[76:79], a[28:31], v[40:43]\n"
    "v_exp_f32 v16, v16\n"
    "v_mfma_f32_16x16x32_bf16 v[44:47], a[80:83], a[16:19], v[76:79]\n"
    "ds_read_b64_tr_b16 a[114:115], v225 offset:5120\n"
    "v_mfma_f32_16x16x32_bf16 v[44:47], a[84:87], a[20:23], v[44:47]\n"
    "v_exp_f32 v17, v17\n"
    "v_mfma_f32_16x16x32_bf16 v[44:47], a[88:91], a[24:27], v[44:47]\n"
    "ds_read_b64_tr_b16 a[116:117], v225 offset:1280\n"
    "v_cvt_pk_bf16_f32 v20, v16, v17\n"
    "v_mfma_f32_16x16x32_bf16 v[44:47], a[92:95], a[28:31], v[44:47]\n"
    "v_exp_f32 v24, v24\n"
    "v_mfma_f32_16x16x32_bf16 v[48:51], a[64:67], a[32:35], v[80:83]\n"
    "ds_read_b64_tr_b16 a[118:119], v225 offset:5376\n"
    "v_mfma_f32_16x16x32_bf16 v[48:51], a[68:71], a[36:39], v[48:51]\n"
    "v_exp_f32 v25, v25\n"
    "v_mfma_f32_16x16x32_bf16 v[48:51], a[72:75], a[40:43], v[48:51]\n"
    "ds_read_b64_tr_b16 a[120:121], v225 offset:1536\n"
    "v_cvt_pk_bf16_f32 v24, v24, v25\n"
    "v_mfma_f32_16x16x32_bf16 v[48:51], a[76:79], a[44:47], v[48:51]\n"
    "v_exp_f32 v26, v26\n"
    "v_mfma_f32_16x16x32_bf16 v[52:55], a[80:83], a[32:35], v[84:87]\n"
    "ds_read_b64_tr_b16 a[122:123], v225 offset:5632\n"
    "v_mfma_f32_16x16x32_bf16 v[52:55], a[84:87], a[36:39], v[52:55]\n"
    "v_exp_f32 v27, v27\n"
    "v_mfma_f32_16x16x32_bf16 v[52:55], a[88:91], a[40:43], v[52:55]\n"
    "ds_read_b64_tr_b16 a[124:125], v225 offset:1792\n"
    "v_cvt_pk_bf16_f32 v25, v26, v27\n"
    "v_mfma_f32_16x16x32_bf16 v[52:55], a[92:95], a[44:47], v[52:55]\n"
    "v_exp_f32 v28, v28\n"
    "v_mfma_f32_16x16x32_bf16 v[56:59], a[64:67], a[48:51], v[88:91]\n"
    "ds_read_b64_tr_b16 a[126:127], v225 offset:5888\n"
    "v_mfma_f32_16x16x32_bf16 v[56:59], a[68:71], a[52:55], v[56:59]\n"
    "v_exp_f32 v29, v29\n"
    "v_mfma_f32_16x16x32_bf16 v[56:59], a[72:75], a[56:59], v[56:59]\n"
    "v_cvt_pk_bf16_f32 v26, v28, v29\n"
    "v_mfma_f32_16x16x32_bf16 v[56:59], a[76:79], a[60:63], v[56:59]\n"
    "v_exp_f32 v30, v30\n"
    "v_mfma_f32_16x16x32_bf16 v[60:63], a[80:83], a[48:51], v[92:95]\n"
    "s_sub_u32 s56, s56, 1\n"
    "v_mfma_f32_16x16x32_bf16 v[60:63], a[84:87], a[52:55], v[60:63]\n"
    "v_exp_f32 v31, v31\n"
    "v_mfma_f32_16x16x32_bf16 v[60:63], a[88:91], a[56:59], v[60:63]\n"
    "v_cvt_pk_bf16_f32 v27, v30, v31\n"
    "v_mfma_f32_16x16x32_bf16 v[60:63], a[92:95], a[60:63], v[60:63]\n"
    "s_waitcnt lgkmcnt(0)\n"
    "s_cbranch_scc1 L4_ev0_%=\n"
    "L4_p1_%=:\n"
    "v_mfma_f32_16x16x32_bf16 v[96:99], a[96:99], v[4:7], v[96:99]\n"
    "v_exp_f32 v38, v38\n"
    "v_mfma_f32_16x16x32_bf16 v[100:103], a[100:103], v[4:7], v[100:103]\n"
    "s_add_u32 m0, s55, s59\n"
    "ds_read_b128 a[64:67], v224 offset:8192\n"
    "v_mfma_f32_16x16x32_bf16 v[104:107], a[104:107], v[4:7], v[104:107]\n"
    "v_exp_f32 v39, v39\n"
    "v_mfma_f32_16x16x32_bf16 v[108:111], a[108:111], v[4:7], v[108:111]\n"
    "v_cvt_pk_bf16_f32 v39, v38, v39\n"
    "v_mfma_f32_16x16x32_bf16 v[112:115], a[112:115], v[4:7], v[112:115]\n"
    "v_exp_f32 v36, v36\n"
    "v_mfma_f32_16x16x32_bf16 v[116:119], a[116:119], v[4:7], v[116:119]\n"
    "ds_read_b128 a[68:71], v224 offset:8704\n"
    "v_mfma_f32_16x16x32_bf16 v[120:123], a[120:123], v[4:7], v[120:123]\n"
    "s_mov_b32 s59, s58\n"
    "v_exp_f32 v37, v37\n"
    "v_mfma_f32_16x16x32_bf16 v[124:127], a[124:127], v[4:7], v[124:127]\n"
    "s_add_u32 s58, s58, 32768\n"
    "ds_read_b128 a[72:75], v224 offset:9216\n"
    "v_cvt_pk_bf16_f32 v38, v36, v37\n"
    "v_mfma_f32_16x16x32_bf16 v[128:131], a[96:99], v[8:11], v[128:131]\n"
    "s_cmp_eq_u32 s58, 98304\n"
    "s_cselect_b32 s58, 0, s58\n"
    "v_exp_f32 v34, v34\n"
    "v_mfma_f32_16x16x32_bf16 v[132:135], a[100:103], v[8:11], v[132:135]\n"
    "buffer_load_dwordx4 v232, s[40:43], s53 offen offset:0 lds\n"
    "v_mfma_f32_16x16x32_bf16 v[136:139], a[104:107], v[8:11], v[136:139]\n"
    "v_exp_f32 v35, v35\n"
    "v_mfma_f32_16x16x32_bf16 v[140:143], a[108:111], v[8:11], v[140:143]\n"
    "ds_read_b128 a[76:79], v224 offset:9728\n"
    "v_cvt_pk_bf16_f32 v37, v34, v35\n"
    "v_mfma_f32_16x16x32_bf16 v[144:147], a[112:115], v[8:11], v[144:147]\n"
    "v_exp_f32 v32, v32\n"
    "v_mfma_f32_16x16x32_bf16 v[148:151], a[116:119], v[8:11], v[148:151]\n"
    "ds_read_b128 a[80:83], v224 offset:12288\n"
    "v_mfma_f32_16x16x32_bf16 v[152:155], a[120:123], v[8:11], v[152:155]\n"
    "v_exp_f32 v33, v33\n"
    "v_mfma_f32_16x16x32_bf16 v[156:159], a[124:127], v[8:11], v[156:159]\n"
    "v_cvt_pk_bf16_f32 v36, v32, v33\n"
    "v_smfmac_f32_16x16x64_bf16 a[152:155], a[192:195], v[4:11], v242\n"
    "v_exp_f32 v40, v40\n"
    "v_mfma_f32_16x16x32_bf16 v[160:163], a[96:99], v[20:23], v[160:163]\n"
    "buffer_load_dwordx4 v233, s[40:43], s53 offen offset:1024 lds\n"
    "v_mfma_f32_16x16x32_bf16 v[164:167], a[100:103], v[20:23], v[164:167]\n"
    "v_exp_f32 v41, v41\n"
    "v_mfma_f32_16x16x32_bf16 v[168:171], a[104:107], v[20:23], v[168:171]\n"
    "ds_read_b128 a[84:87], v224 offset:12800\n"
    "v_cvt_pk_bf16_f32 v40, v40, v41\n"
    "v_mfma_f32_16x16x32_bf16 v[172:175], a[108:111], v[20:23], v[172:175]\n"
    "v_exp_f32 v42, v42\n"
    "v_mfma_f32_16x16x32_bf16 v[176:179], a[112:115], v[20:23], v[176:179]\n"
    "ds_read_b128 a[88:91], v224 offset:13312\n"
    "v_mfma_f32_16x16x32_bf16 v[180:183], a[116:119], v[20:23], v[180:183]\n"
    "v_exp_f32 v43, v43\n"
    "v_mfma_f32_16x16x32_bf16 v[184:187], a[120:123], v[20:23], v[184:187]\n"
    "buffer_load_dwordx4 v234, s[40:43], s53 offen offset:2048 lds\n"
    "v_mfma_f32_16x16x32_bf16 v[188:191], a[124:127], v[20:23], v[188:191]\n"
    "v_exp_f32 v44, v44\n"
    "v_mfma_f32_16x16x32_bf16 v[192:195], a[96:99], v[24:27], v[192:195]\n"
    "ds_read_b128 a[92:95], v224 offset:13824\n"
    "v_cvt_pk_bf16_f32 v41, v42, v43\n"
    "v_mfma_f32_16x16x32_bf16 v[196:199], a[100:103], v[24:27], v[196:199]\n"
    "v_exp_f32 v45, v45\n"
    "v_mfma_f32_16x16x32_bf16 v[200:203], a[104:107], v[24:27], v[200:203]\n"
    "ds_read_b64_tr_b16 a[96:97], v225 offset:8192\n"
    "v_cvt_pk_bf16_f32 v42, v44, v45\n"
    "v_mfma_f32_16x16x32_bf16 v[204:207], a[108:111], v[24:27], v[204:207]\n"
    "v_exp_f32 v46, v46\n"
    "v_mfma_f32_16x16x32_bf16 v[208:211], a[112:115], v[24:27], v[208:211]\n"
    "buffer_load_dwordx4 v235, s[40:43], s53 offen offset:3072 lds\n"
    "v_mfma_f32_16x16x32_bf16 v[212:215], a[116:119], v[24:27], v[212:215]\n"
    "v_exp_f32 v47, v47\n"
    "v_mfma_f32_16x16x32_bf16 v[216:219], a[120:123], v[24:27], v[216:219]\n"
    "ds_read_b64_tr_b16 a[98:99], v225 offset:12288\n"
    "v_cvt_pk_bf16_f32 v43, v46, v47\n"
    "v_mfma_f32_16x16x32_bf16 v[220:223], a[124:127], v[24:27], v[220:223]\n"
    "ds_read_b64_tr_b16 a[100:101], v225 offset:8448\n"
    "ds_read_b64_tr_b16 a[102:103], v225 offset:12544\n"
    "v_smfmac_f32_16x16x64_bf16 a[156:159], a[192:195], v[20:27], v242\n"
    "s_waitcnt lgkmcnt(4)\n"
    "v_exp_f32 v54, v54\n"
    "v_mfma_f32_16x16x32_bf16 v[0:3], a[64:67], a[0:3], v[64:67]\n"
    "s_add_u32 s53, s53, 0x40000\n"
    "ds_read_b64_tr_b16 a[104:105], v225 offset:8704\n"
    "v_add_u32 v224, s58, v226\n"
    "v_mfma_f32_16x16x32_bf16 v[0:3], a[68:71], a[4:7], v[0:3]\n"
    "v_exp_f32 v55, v55\n"
    "v_mfma_f32_16x16x32_bf16 v[0:3], a[72:75], a[8:11], v[0:3]\n"
    "ds_read_b64_tr_b16 a[106:107], v225 offset:12800\n"
    "v_cvt_pk_bf16_f32 v55, v54, v55\n"
    "v_mfma_f32_16x16x32_bf16 v[0:3], a[76:79], a[12:15], v[0:3]\n"
    "v_exp_f32 v52, v52\n"
    "v_mfma_f32_16x16x32_bf16 v[4:7], a[80:83], a[0:3], v[68:71]\n"
    "ds_read_b64_tr_b16 a[108:109], v225 offset:8960\n"
    "v_mfma_f32_16x16x32_bf16 v[4:7], a[84:87], a[4:7], v[4:7]\n"
    "v_exp_f32 v53, v53\n"
    "v_mfma_f32_16x16x32_bf16 v[4:7], a[88:91], a[8:11], v[4:7]\n"
    "ds_read_b64_tr_b16 a[110:111], v225 offset:13056\n"
    "v_cvt_pk_bf16_f32 v54, v52, v53\n"
    "v_mfma_f32_16x16x32_bf16 v[4:7], a[92:95], a[12:15], v[4:7]\n"
    "v_exp_f32 v50, v50\n"
    "v_mfma_f32_16x16x32_bf16 v[8:11], a[64:67], a[16:19], v[72:75]\n"
    "v_mfma_f32_16x16x32_bf16 v[8:11], a[68:71], a[20:23], v[8:11]\n"
    "v_exp_f32 v51, v51\n"
    "v_mfma_f32_16x16x32_bf16 v[8:11], a[72:75], a[24:27], v[8:11]\n"
    "ds_read_b64_tr_b16 a[112:113], v225 offset:9216\n"
    "v_cvt_pk_bf16_f32 v53, v50, v51\n"
    "v_mfma_f32_16x16x32_bf16 v[8:11], a[76:79], a[28:31], v[8:11]\n"
    "v_exp_f32 v48, v48\n"
    "v_mfma_f32_16x16x32_bf16 v[12:15], a[80:83], a[16:19], v[76:79]\n"
    "ds_read_b64_tr_b16 a[114:115], v225 offset:13312\n"
    "v_mfma_f32_16x16x32_bf16 v[12:15], a[84:87], a[20:23], v[12:15]\n"
    "v_exp_f32 v49, v49\n"
    "v_mfma_f32_16x16x32_bf16 v[12:15], a[88:91], a[24:27], v[12:15]\n"
    "ds_read_b64_tr_b16 a[116:117], v225 offset:9472\n"
    "v_cvt_pk_bf16_f32 v52, v48, v49\n"
    "v_mfma_f32_16x16x32_bf16 v[12:15], a[92:95], a[28:31], v[12:15]\n"
    "v_exp_f32 v56, v56\n"
    "v_mfma_f32_16x16x32_bf16 v[16:19], a[64:67], a[32:35], v[80:83]\n"
    "ds_read_b64_tr_b16 a[118:119], v225 offset:13568\n"
    "v_mfma_f32_16x16x32_bf16 v[16:19], a[68:71], a[36:39], v[16:19]\n"
    "v_exp_f32 v57, v57\n"
    "v_mfma_f32_16x16x32_bf16 v[16:19], a[72:75], a[40:43], v[16:19]\n"
    "ds_read_b64_tr_b16 a[120:121], v225 offset:9728\n"
    "v_cvt_pk_bf16_f32 v56, v56, v57\n"
    "v_mfma_f32_16x16x32_bf16 v[16:19], a[76:79], a[44:47], v[16:19]\n"
    "v_exp_f32 v58, v58\n"
    "v_mfma_f32_16x16x32_bf16 v[20:23], a[80:83], a[32:35], v[84:87]\n"
    "ds_read_b64_tr_b16 a[122:123], v225 offset:13824\n"
    "v_mfma_f32_16x16x32_bf16 v[20:23], a[84:87], a[36:39], v[20:23]\n"
    "v_exp_f32 v59, v59\n"
    "v_mfma_f32_16x16x32_bf16 v[20:23], a[88:91], a[40:43], v[20:23]\n"
    "ds_read_b64_tr_b16 a[124:125], v225 offset:9984\n"
    "v_cvt_pk_bf16_f32 v57, v58, v59\n"
    "v_mfma_f32_16x16x32_bf16 v[20:23], a[92:95], a[44:47], v[20:23]\n"
    "v_exp_f32 v60, v60\n"
    "v_mfma_f32_16x16x32_bf16 v[24:27], a[64:67], a[48:51], v[88:91]\n"
    "ds_read_b64_tr_b16 a[126:127], v225 offset:14080\n"
    "v_mfma_f32_16x16x32_bf16 v[24:27], a[68:71], a[52:55], v[24:27]\n"
    "v_exp_f32 v61, v61\n"
    "v_mfma_f32_16x16x32_bf16 v[24:27], a[72:75], a[56:59], v[24:27]\n"
    "v_cvt_pk_bf16_f32 v58, v60, v61\n"
    "v_mfma_f32_16x16x32_bf16 v[24:27], a[76:79], a[60:63], v[24:27]\n"
    "v_exp_f32 v62, v62\n"
    "v_mfma_f32_16x16x32_bf16 v[28:31], a[80:83], a[48:51], v[92:95]\n"
    "s_sub_u32 s56, s56, 1\n"
    "v_mfma_f32_16x16x32_bf16 v[28:31], a[84:87], a[52:55], v[28:31]\n"
    "v_exp_f32 v63, v63\n"
    "v_mfma_f32_16x16x32_bf16 v[28:31], a[88:91], a[56:59], v[28:31]\n"
    "v_cvt_pk_bf16_f32 v59, v62, v63\n"
    "v_add_u32 v225, s58, v227\n"
    "v_mfma_f32_16x16x32_bf16 v[28:31], a[92:95], a[60:63], v[28:31]\n"
    "s_waitcnt lgkmcnt(0)\n"
    "s_cbranch_scc0 L4_p0_%=\n"
    "s_cmp_eq_u32 s57, 0\n"
    "s_cbranch_scc0 L4_fix1_%=\n"
    "L4_tail0_%=:\n"
    "v_mfma_f32_16x16x32_bf16 v[96:99], a[96:99], v[36:39], v[96:99]\n"
    "s_waitcnt vmcnt(8)\n"
    "s_barrier\n"
    "v_mfma_f32_16x16x32_bf16 v[100:103], a[100:103], v[36:39], v[100:103]\n"
    "s_mov_b32 s61, 0x5f800000\n"
    "ds_read_b64_tr_b16 a[64:65], v225 offset:0\n"
    "ds_read_b64_tr_b16 a[66:67], v225 offset:4096\n"
    "v_mfma_f32_16x16x32_bf16 v[104:107], a[104:107], v[36:39], v[104:107]\n"
    "s_mov_b64 s[76:77], 0\n"
    "v_exp_f32 v6, v6\n"
    "v_mfma_f32_16x16x32_bf16 v[108:111], a[108:111], v[36:39], v[108:111]\n"
    "ds_read_b64_tr_b16 a[68:69], v225 offset:256\n"
    "ds_read_b64_tr_b16 a[70:71], v225 offset:4352\n"
    "v_mfma_f32_16x16x32_bf16 v[112:115], a[112:115], v[36:39], v[112:115]\n"
    "v_exp_f32 v7, v7\n"
    "v_mfma_f32_16x16x32_bf16 v[116:119], a[116:119], v[36:39], v[116:119]\n"
    "ds_read_b64_tr_b16 a[72:73], v225 offset:512\n"
    "ds_read_b64_tr_b16 a[74:75], v225 offset:4608\n"
    "v_mfma_f32_16x16x32_bf16 v[120:123], a[120:123], v[36:39], v[120:123]\n"
    "v_exp_f32 v4, v4\n"
    "v_mfma_f32_16x16x32_bf16 v[124:127], a[124:127], v[36:39], v[124:127]\n"
    "ds_read_b64_tr_b16 a[76:77], v225 offset:768\n"
    "ds_read_b64_tr_b16 a[78:79], v225 offset:4864\n"
    "v_mfma_f32_16x16x32_bf16 a[128:131], a[144:147], v[36:39], a[128:131]\n"
    "v_exp_f32 v5, v5\n"
    "v_mfma_f32_16x16x32_bf16 v[128:131], a[96:99], v[40:43], v[128:131]\n"
    "ds_read_b64_tr_b16 a[80:81], v225 offset:1024\n"
    "ds_read_b64_tr_b16 a[82:83], v225 offset:5120\n"
    "v_mfma_f32_16x16x32_bf16 v[132:135], a[100:103], v[40:43], v[132:135]\n"
    "v_cvt_pk_bf16_f32 v7, v6, v7\n"
    "v_cvt_pk_bf16_f32 v6, v4, v5\n"
    "v_mfma_f32_16x16x32_bf16 v[136:139], a[104:107], v[40:43], v[136:139]\n"
    "ds_read_b64_tr_b16 a[84:85], v225 offset:1280\n"
    "ds_read_b64_tr_b16 a[86:87], v225 offset:5376\n"
    "v_mfma_f32_16x16x32_bf16 v[140:143], a[108:111], v[40:43], v[140:143]\n"
    "v_exp_f32 v2, v2\n"
    "v_mfma_f32_16x16x32_bf16 v[144:147], a[112:115], v[40:43], v[144:147]\n"
    "ds_read_b64_tr_b16 a[88:89], v225 offset:1536\n"
    "ds_read_b64_tr_b16 a[90:91], v225 offset:5632\n"
    "v_mfma_f32_16x16x32_bf16 v[148:151], a[116:119], v[40:43], v[148:151]\n"
    "v_exp_f32 v3, v3\n"
    "v_mfma_f32_16x16x32_bf16 v[152:155], a[120:123], v[40:43], v[152:155]\n"
    "ds_read_b64_tr_b16 a[92:93], v225 offset:1792\n"
    "ds_read_b64_tr_b16 a[94:95], v225 offset:5888\n"
    "v_mfma_f32_16x16x32_bf16 v[156:159], a[124:127], v[40:43], v[156:159]\n"
    "v_exp_f32 v0, v0\n"
    "v_mfma_f32_16x16x32_bf16 a[132:135], a[144:147], v[40:43], a[132:135]\n"
    "v_exp_f32 v1, v1\n"
    "v_mfma_f32_16x16x32_bf16 v[160:163], a[96:99], v[52:55], v[160:163]\n"
    "v_cvt_pk_bf16_f32 v5, v2, v3\n"
    "v_cvt_pk_bf16_f32 v4, v0, v1\n"
    "v_mfma_f32_16x16x32_bf16 v[164:167], a[100:103], v[52:55], v[164:167]\n"
    "v_exp_f32 v8, v8\n"
    "v_mfma_f32_16x16x32_bf16 v[168:171], a[104:107], v[52:55], v[168:171]\n"
    "v_exp_f32 v9, v9\n"
    "v_mfma_f32_16x16x32_bf16 v[172:175], a[108:111], v[52:55], v[172:175]\n"
    "v_exp_f32 v10, v10\n"
    "v_mfma_f32_16x16x32_bf16 v[176:179], a[112:115], v[52:55], v[176:179]\n"
    "v_exp_f32 v11, v11\n"
    "v_mfma_f32_16x16x32_bf16 v[180:183], a[116:119], v[52:55], v[180:183]\n"
    "v_cvt_pk_bf16_f32 v8, v8, v9\n"
    "v_cvt_pk_bf16_f32 v9, v10, v11\n"
    "v_mfma_f32_16x16x32_bf16 v[184:187], a[120:123], v[52:55], v[184:187]\n"
    "v_exp_f32 v12, v12\n"
    "v_mfma_f32_16x16x32_bf16 v[188:191], a[124:127], v[52:55], v[188:191]\n"
    "v_exp_f32 v13, v13\n"
    "v_mfma_f32_16x16x32_bf16 a[136:139], a[144:147], v[52:55], a[136:139]\n"
    "v_exp_f32 v14, v14\n"
    "v_mfma_f32_16x16x32_bf16 v[192:195], a[96:99], v[56:59], v[192:195]\n"
    "v_exp_f32 v15, v15\n"
    "v_mfma_f32_16x16x32_bf16 v[196:199], a[100:103], v[56:59], v[196:199]\n"
    "v_cvt_pk_bf16_f32 v10, v12, v13\n"
    "v_cvt_pk_bf16_f32 v11, v14, v15\n"
    "v_mfma_f32_16x16x32_bf16 v[200:203], a[104:107], v[56:59], v[200:203]\n"
    "v_exp_f32 v22, v22\n"
    "v_mfma_f32_16x16x32_bf16 v[204:207], a[108:111], v[56:59], v[204:207]\n"
    "v_exp_f32 v23, v23\n"
    "v_mfma_f32_16x16x32_bf16 v[208:211], a[112:115], v[56:59], v[208:211]\n"
    "v_exp_f32 v20, v20\n"
    "v_mfma_f32_16x16x32_bf16 v[212:215], a[116:119], v[56:59], v[212:215]\n"
    "v_exp_f32 v21, v21\n"
    "v_mfma_f32_16x16x32_bf16 v[216:219], a[120:123], v[56:59], v[216:219]\n"
    "v_cvt_pk_bf16_f32 v23, v22, v23\n"
    "v_cvt_pk_bf16_f32 v22, v20, v21\n"
    "v_mfma_f32_16x16x32_bf16 v[220:223], a[124:127], v[56:59], v[220:223]\n"
    "v_exp_f32 v18, v18\n"
    "v_mfma_f32_16x16x32_bf16 a[140:143], a[144:147], v[56:59], a[140:143]\n"
    "v_exp_f32 v19, v19\n"
    "s_waitcnt lgkmcnt(0)\n"
    "v_mfma_f32_16x16x32_bf16 a[128:131], a[144:147], v[4:7], a[128:131]\n"
    "v_exp_f32 v16, v16\n"
    "v_mfma_f32_16x16x32_bf16 v[96:99], a[64:67], v[4:7], v[96:99]\n"
    "v_exp_f32 v17, v17\n"
    "v_mfma_f32_16x16x32_bf16 v[100:103], a[68:71], v[4:7], v[100:103]\n"
    "v_cvt_pk_bf16_f32 v21, v18, v19\n"
    "v_cvt_pk_bf16_f32 v20, v16, v17\n"
    "v_mfma_f32_16x16x32_bf16 v[104:107], a[72:75], v[4:7], v[104:107]\n"
    "v_exp_f32 v24, v24\n"
    "v_mfma_f32_16x16x32_bf16 v[108:111], a[76:79], v[4:7], v[108:111]\n"
    "v_exp_f32 v25, v25\n"
    "v_mfma_f32_16x16x32_bf16 v[112:115], a[80:83], v[4:7], v[112:115]\n"
    "v_exp_f32 v26, v26\n"
    "v_mfma_f32_16x16x32_bf16 v[116:119], a[84:87], v[4:7], v[116:119]\n"
    "v_exp_f32 v27, v27\n"
    "v_mfma_f32_16x16x32_bf16 v[120:123], a[88:91], v[4:7], v[120:123]\n"
    "v_cvt_pk_bf16_f32 v24, v24, v25\n"
    "v_cvt_pk_bf16_f32 v25, v26, v27\n"
    "v_mfma_f32_16x16x32_bf16 v[124:127], a[92:95], v[4:7], v[124:127]\n"
    "v_exp_f32 v28, v28\n"
    "s_nop 1\n"
    "v_mfma_f32_16x16x32_bf16 a[132:135], a[144:147], v[8:11], a[132:135]\n"
    "v_exp_f32 v29, v29\n"
    "v_mfma_f32_16x16x32_bf16 v[128:131], a[64:67], v[8:11], v[128:131]\n"
    "v_exp_f32 v30, v30\n"
    "v_mfma_f32_16x16x32_bf16 v[132:135], a[68:71], v[8:11], v[132:135]\n"
    "v_exp_f32 v31, v31\n"
    "v_mfma_f32_16x16x32_bf16 v[136:139], a[72:75], v[8:11], v[136:139]\n"
    "v_cvt_pk_bf16_f32 v26, v28, v29\n"
    "v_cvt_pk_bf16_f32 v27, v30, v31\n"
    "v_mfma_f32_16x16x32_bf16 v[140:143], a[76:79], v[8:11], v[140:143]\n"
    "v_accvgpr_read_b32 v88, a128\n"
    "v_accvgpr_read_b32 v64, a152\n"
    "v_mfma_f32_16x16x32_bf16 v[144:147], a[80:83], v[8:11], v[144:147]\n"
    "v_accvgpr_read_b32 v65, a153\n"
    "v_add_f32 v88, v88, v64\n"
    "v_mfma_f32_16x16x32_bf16 v[148:151], a[84:87], v[8:11], v[148:151]\n"
    "v_add_f32 v88, v88, v65\n"
    "v_cmp_nge_f32 vcc, s61, v88\n"
    "v_mfma_f32_16x16x32_bf16 v[152:155], a[88:91], v[8:11], v[152:155]\n"
    "v_rcp_f32 v64, v88\n"
    "v_mfma_f32_16x16x32_bf16 v[156:159], a[92:95], v[8:11], v[156:159]\n"
    "s_or_b64 s[76:77], s[76:77], vcc\n"
    "v_mov_b32 v65, v64\n"
    "v_mfma_f32_16x16x32_bf16 a[136:139], a[144:147], v[20:23], a[136:139]\n"
    "v_mul_f32 v96, v96, v64\n"
    "v_mul_f32 v97, v97, v64\n"
    "v_mfma_f32_16x16x32_bf16 v[160:163], a[64:67], v[20:23], v[160:163]\n"
    "v_mul_f32 v98, v98, v64\n"
    "v_mul_f32 v99, v99, v64\n"
    "v_mfma_f32_16x16x32_bf16 v[164:167], a[68:71], v[20:23], v[164:167]\n"
    "v_cvt_pk_bf16_f32 v96, v96, v97\n"
    "v_cvt_pk_bf16_f32 v97, v98, v99\n"
    "v_mfma_f32_16x16x32_bf16 v[168:171], a[72:75], v[20:23], v[168:171]\n"
    "ds_write_b64 v251, v[96:97] offset:0\n"
    "v_mul_f32 v100, v100, v64\n"
    "v_mfma_f32_16x16x32_bf16 v[172:175], a[76:79], v[20:23], v[172:175]\n"
    "v_mul_f32 v101, v101, v64\n"
    "v_mul_f32 v102, v102, v64\n"
    "v_mfma_f32_16x16x32_bf16 v[176:179], a[80:83], v[20:23], v[176:179]\n"
    "v_mul_f32 v103, v103, v64\n"
    "v_cvt_pk_bf16_f32 v100, v100, v101\n"
    "v_mfma_f32_16x16x32_bf16 v[180:183], a[84:87], v[20:23], v[180:183]\n"
    "v_cvt_pk_bf16_f32 v101, v102, v103\n"
    "v_mul_f32 v104, v104, v64\n"
    "v_mfma_f32_16x16x32_bf16 v[184:187], a[88:91], v[20:23], v[184:187]\n"
    "ds_write_b64 v251, v[100:101] offset:32\n"
    "v_mul_f32 v105, v105, v64\n"
    "v_mfma_f32_16x16x32_bf16 v[188:191], a[92:95], v[20:23], v[188:191]\n"
    "v_mul_f32 v106, v106, v64\n"
    "v_mul_f32 v107, v107, v64\n"
    "v_mfma_f32_16x16x32_bf16 a[140:143], a[144:147], v[24:27], a[140:143]\n"
    "v_cvt_pk_bf16_f32 v104, v104, v105\n"
    "v_cvt_pk_bf16_f32 v105, v106, v107\n"
    "v_mfma_f32_16x16x32_bf16 v[192:195], a[64:67], v[24:27], v[192:195]\n"
    "ds_write_b64 v251, v[104:105] offset:64\n"
    "v_mul_f32 v108, v108, v64\n"
    "v_mfma_f32_16x16x32_bf16 v[196:199], a[68:71], v[24:27], v[196:199]\n"
    "v_mul_f32 v109, v109, v64\n"
    "v_mul_f32 v110, v110, v64\n"
    "v_mfma_f32_16x16x32_bf16 v[200:203], a[72:75], v[24:27], v[200:203]\n"
    "v_mul_f32 v111, v111, v64\n"
    "v_cvt_pk_bf16_f32 v108, v108, v109\n"
    "v_mfma_f32_16x16x32_bf16 v[204:207], a[76:79], v[24:27], v[204:207]\n"
    "v_cvt_pk_bf16_f32 v109, v110, v111\n"
    "v_mul_f32 v112, v112, v64\n"
    "v_mfma_f32_16x16x32_bf16 v[208:211], a[80:83], v[24:27], v[208:211]\n"
    "ds_write_b64 v251, v[108:109] offset:96\n"
    "v_mul_f32 v113, v113, v64\n"
    "v_mfma_f32_16x16x32_bf16 v[212:215], a[84:87], v[24:27], v[212:215]\n"
    "v_mul_f32 v114, v114, v64\n"
    "v_mul_f32 v115, v115, v64\n"
    "v_mfma_f32_16x16x32_bf16 v[216:219], a[88:91], v[24:27], v[216:219]\n"
    "v_cvt_pk_bf16_f32 v112, v112, v113\n"
    "v_cvt_pk_bf16_f32 v113, v114, v115\n"
    "v_mfma_f32_16x16x32_bf16 v[220:223], a[92:95], v[24:27], v[220:223]\n"
    "ds_write_b64 v251, v[112:113] offset:128\n"
    "v_mul_f32 v116, v116, v64\n"
    "v_mul_f32 v117, v117, v64\n"
    "v_mul_f32 v118, v118, v64\n"
    "v_mul_f32 v119, v119, v64\n"
    "v_cvt_pk_bf16_f32 v116, v116, v117\n"
    "v_cvt_pk_bf16_f32 v117, v118, v119\n"
    "v_pk_mul_f32 v[120:121], v[120:121], v[64:65]\n"
    "v_pk_mul_f32 v[122:123], v[122:123], v[64:65]\n"
    "v_cvt_pk_bf16_f32 v120, v120, v121\n"
    "v_cvt_pk_bf16_f32 v121, v122, v123\n"
    "v_pk_mul_f32 v[124:125], v[124:125], v[64:65]\n"
    "v_pk_mul_f32 v[126:127], v[126:127], v[64:65]\n"
    "v_cvt_pk_bf16_f32 v124, v124, v125\n"
    "v_cvt_pk_bf16_f32 v125, v126, v127\n"
    "ds_write_b64 v251, v[116:117] offset:160\n"
    "ds_write_b64 v251, v[120:121] offset:192\n"
    "ds_write_b64 v251, v[124:125] offset:224\n"
    "ds_read_b128 v[32:35], v252 offset:0\n"
    "ds_read_b128 v[36:39], v252 offset:1088\n"
    "ds_read_b128 v[40:43], v252 offset:2176\n"
    "ds_read_b128 v[44:47], v252 offset:3264\n"
    "v_accvgpr_read_b32 v89, a132\n"
    "v_accvgpr_read_b32 v66, a154\n"
    "v_accvgpr_read_b32 v67, a155\n"
    "v_add_f32 v89, v89, v66\n"
    "v_add_f32 v89, v89, v67\n"
    "v_cmp_nge_f32 vcc, s61, v89\n"
    "v_rcp_f32 v66, v89\n"
    "s_or_b64 s[76:77], s[76:77], vcc\n"
    "v_mov_b32 v67, v66\n"
    "v_pk_mul_f32 v[128:129], v[128:129], v[66:67]\n"
    "v_pk_mul_f32 v[130:131], v[130:131], v[66:67]\n"
    "v_cvt_pk_bf16_f32 v128, v128, v129\n"
    "v_cvt_pk_bf16_f32 v129, v130, v131\n"
    "v_pk_mul_f32 v[132:133], v[132:133], v[66:67]\n"
    "v_pk_mul_f32 v[134:135], v[134:135], v[66:67]\n"
    "v_cvt_pk_bf16_f32 v132, v132, v133\n"
    "v_cvt_pk_bf16_f32 v133, v134, v135\n"
    "s_mov_b32 s53, 0\n"
    "s_waitcnt lgkmcnt(3)\n"
    "buffer_store_dwordx4 v[32:35], v253, s[48:51], s53 offen nt\n"
    "ds_write_b64 v251, v[128:129] offset:4352\n"
    "ds_write_b64 v251, v[132:133] offset:4384\n"
    "v_pk_mul_f32 v[136:137], v[136:137], v[66:67]\n"
    "v_pk_mul_f32 v[138:139], v[138:139], v[66:67]\n"
    "v_cvt_pk_bf16_f32 v136, v136, v137\n"
    "v_cvt_pk_bf16_f32 v137, v138, v139\n"
    "v_pk_mul_f32 v[140:141], v[140:141], v[66:67]\n"
    "v_pk_mul_f32 v[142:143], v[142:143], v[66:67]\n"
    "v_cvt_pk_bf16_f32 v140, v140, v141\n"
    "v_cvt_pk_bf16_f32 v141, v142, v143\n"
    "s_mov_b32 s53, 16384\n"
    "s_waitcnt lgkmcnt(4)\n"
    "buffer_store_dwordx4 v[36:39], v253, s[48:51], s53 offen nt\n"
    "ds_write_b64 v251, v[136:137] offset:4416\n"
    "ds_write_b64 v251, v[140:141] offset:4448\n"
    "v_pk_mul_f32 v[144:145], v[144:145], v[66:67]\n"
    "v_pk_mul_f32 v[146:147], v[146:147], v[66:67]\n"
    "v_cvt_pk_bf16_f32 v144, v144, v145\n"
    "v_cvt_pk_bf16_f32 v145, v146, v147\n"
    "v_pk_mul_f32 v[148:149], v[148:149], v[66:67]\n"
    "v_pk_mul_f32 v[150:151], v[150:151], v[66:67]\n"
    "v_cvt_pk_bf16_f32 v148, v148, v149\n"
    "v_cvt_pk_bf16_f32 v149, v150, v151\n"
    "s_mov_b32 s53, 32768\n"
    "s_waitcnt lgkmcnt(5)\n"
    "buffer_store_dwordx4 v[40:43], v253, s[48:51], s53 offen nt\n"
    "ds_write_b64 v251, v[144:145] offset:4480\n"
    "ds_write_b64 v251, v[148:149] offset:4512\n"
    "v_pk_mul_f32 v[152:153], v[152:153], v[66:67]\n"
    "v_pk_mul_f32 v[154:155], v[154:155], v[66:67]\n"
    "v_cvt_pk_bf16_f32 v152, v152, v153\n"
    "v_cvt_pk_bf16_f32 v153, v154, v155\n"
    "v_pk_mul_f32 v[156:157], v[156:157], v[66:67]\n"
    "v_pk_mul_f32 v[158:159], v[158:159], v[66:67]\n"
    "v_cvt_pk_bf16_f32 v156, v156, v157\n"
    "v_cvt_pk_bf16_f32 v157, v158, v159\n"
    "s_mov_b32 s53, 49152\n"
    "s_waitcnt lgkmcnt(6)\n"
    "buffer_store_dwordx4 v[44:47], v253, s[48:51], s53 offen nt\n"
    "ds_write_b64 v251, v[152:153] offset:4544\n"
    "ds_write_b64 v251, v[156:157] offset:4576\n"
    "ds_read_b128 v[48:51], v252 offset:4352\n"
    "ds_read_b128 v[52:55], v252 offset:5440\n"
    "ds_read_b128 v[56:59], v252 offset:6528\n"
    "ds_read_b128 v[60:63], v252 offset:7616\n"
    "v_accvgpr_read_b32 v90, a136\n"
    "v_accvgpr_read_b32 v68, a156\n"
    "v_accvgpr_read_b32 v69, a157\n"
    "v_add_f32 v90, v90, v68\n"
    "v_add_f32 v90, v90, v69\n"
    "v_cmp_nge_f32 vcc, s61, v90\n"
    "v_rcp_f32 v68, v90\n"
    "s_or_b64 s[76:77], s[76:77], vcc\n"
    "v_mov_b32 v69, v68\n"
    "v_pk_mul_f32 v[160:161], v[160:161], v[68:69]\n"
    "v_pk_mul_f32 v[162:163], v[162:163], v[68:69]\n"
    "v_cvt_pk_bf16_f32 v160, v160, v161\n"
    "v_cvt_pk_bf16_f32 v161, v162, v163\n"
    "v_pk_mul_f32 v[164:165], v[164:165], v[68:69]\n"
    "v_pk_mul_f32 v[166:167], v[166:167], v[68:69]\n"
    "v_cvt_pk_bf16_f32 v164, v164, v165\n"
    "v_cvt_pk_bf16_f32 v165, v166, v167\n"
    "s_mov_b32 s53, 65536\n"
    "s_waitcnt lgkmcnt(3)\n"
    "buffer_store_dwordx4 v[48:51], v253, s[48:51], s53 offen nt\n"
    "ds_write_b64 v251, v[160:161] offset:8704\n"
    "ds_write_b64 v251, v[164:165] offset:8736\n"
    "v_pk_mul_f32 v[168:169], v[168:169], v[68:69]\n"
    "v_pk_mul_f32 v[170:171], v[170:171], v[68:69]\n"
    "v_cvt_pk_bf16_f32 v168, v168, v169\n"
    "v_cvt_pk_bf16_f32 v169, v170, v171\n"
    "v_pk_mul_f32 v[172:173], v[172:173], v[68:69]\n"
    "v_pk_mul_f32 v[174:175], v[174:175], v[68:69]\n"
    "v_cvt_pk_bf16_f32 v172, v172, v173\n"
    "v_cvt_pk_bf16_f32 v173, v174, v175\n"
    "s_mov_b32 s53, 81920\n"
    "s_waitcnt lgkmcnt(4)\n"
    "buffer_store_dwordx4 v[52:55], v253, s[48:51], s53 offen nt\n"
    "ds_write_b64 v251, v[168:169] offset:8768\n"
    "ds_write_b64 v251, v[172:173] offset:8800\n"
    "v_pk_mul_f32 v[176:177], v[176:177], v[68:69]\n"
    "v_pk_mul_f32 v[178:179], v[178:179], v[68:69]\n"
    "v_cvt_pk_bf16_f32 v176, v176, v177\n"
    "v_cvt_pk_bf16_f32 v177, v178, v179\n"
    "v_pk_mul_f32 v[180:181], v[180:181], v[68:69]\n"
    "v_pk_mul_f32 v[182:183], v[182:183], v[68:69]\n"
    "v_cvt_pk_bf16_f32 v180, v180, v181\n"
    "v_cvt_pk_bf16_f32 v181, v182, v183\n"
    "s_mov_b32 s53, 98304\n"
    "s_waitcnt lgkmcnt(5)\n"
    "buffer_store_dwordx4 v[56:59], v253, s[48:51], s53 offen nt\n"
    "ds_write_b64 v251, v[176:177] offset:8832\n"
    "ds_write_b64 v251, v[180:181] offset:8864\n"
    "v_pk_mul_f32 v[184:185], v[184:185], v[68:69]\n"
    "v_pk_mul_f32 v[186:187], v[186:187], v[68:69]\n"
    "v_cvt_pk_bf16_f32 v184, v184, v185\n"
    "v_cvt_pk_bf16_f32 v185, v186, v187\n"
    "v_pk_mul_f32 v[188:189], v[188:189], v[68:69]\n"
    "v_pk_mul_f32 v[190:191], v[190:191], v[68:69]\n"
    "v_cvt_pk_bf16_f32 v188, v188, v189\n"
    "v_cvt_pk_bf16_f32 v189, v190, v191\n"
    "s_mov_b32 s53, 114688\n"
    "s_waitcnt lgkmcnt(6)\n"
    "buffer_store_dwordx4 v[60:63], v253, s[48:51], s53 offen nt\n"
    "ds_write_b64 v251, v[184:185] offset:8896\n"
    "ds_write_b64 v251, v[188:189] offset:8928\n"
    "ds_read_b128 v[32:35], v252 offset:8704\n"
    "ds_read_b128 v[36:39], v252 offset:9792\n"
    "ds_read_b128 v[40:43], v252 offset:10880\n"
    "ds_read_b128 v[44:47], v252 offset:11968\n"
    "v_accvgpr_read_b32 v91, a140\n"
    "v_accvgpr_read_b32 v70, a158\n"
    "v_accvgpr_read_b32 v71, a159\n"
    "v_add_f32 v91, v91, v70\n"
    "v_add_f32 v91, v91, v71\n"
    "v_cmp_nge_f32 vcc, s61, v91\n"
    "v_rcp_f32 v70, v91\n"
    "s_or_b64 s[76:77], s[76:77], vcc\n"
    "v_mov_b32 v71, v70\n"
    "v_pk_mul_f32 v[192:193], v[192:193], v[70:71]\n"
    "v_pk_mul_f32 v[194:195], v[194:195], v[70:71]\n"
    "v_cvt_pk_bf16_f32 v192, v192, v193\n"
    "v_cvt_pk_bf16_f32 v193, v194, v195\n"
    "v_pk_mul_f32 v[196:197], v[196:197], v[70:71]\n"
    "v_pk_mul_f32 v[198:199], v[198:199], v[70:71]\n"
    "v_cvt_pk_bf16_f32 v196, v196, v197\n"
    "v_cvt_pk_bf16_f32 v197, v198, v199\n"
    "s_mov_b32 s53, 131072\n"
    "s_waitcnt lgkmcnt(3)\n"
    "buffer_store_dwordx4 v[32:35], v253, s[48:51], s53 offen nt\n"
    "ds_write_b64 v251, v[192:193] offset:0\n"
    "ds_write_b64 v251, v[196:197] offset:32\n"
    "v_pk_mul_f32 v[200:201], v[200:201], v[70:71]\n"
    "v_pk_mul_f32 v[202:203], v[202:203], v[70:71]\n"
    "v_cvt_pk_bf16_f32 v200, v200, v201\n"
    "v_cvt_pk_bf16_f32 v201, v202, v203\n"
    "v_pk_mul_f32 v[204:205], v[204:205], v[70:71]\n"
    "v_pk_mul_f32 v[206:207], v[206:207], v[70:71]\n"
    "v_cvt_pk_bf16_f32 v204, v204, v205\n"
    "v_cvt_pk_bf16_f32 v205, v206, v207\n"
    "s_mov_b32 s53, 147456\n"
    "s_waitcnt lgkmcnt(4)\n"
    "buffer_store_dwordx4 v[36:39], v253, s[48:51], s53 offen nt\n"
    "ds_write_b64 v251, v[200:201] offset:64\n"
    "ds_write_b64 v251, v[204:205] offset:96\n"
    "v_pk_mul_f32 v[208:209], v[208:209], v[70:71]\n"
    "v_pk_mul_f32 v[210:211], v[210:211], v[70:71]\n"
    "v_cvt_pk_bf16_f32 v208, v208, v209\n"
    "v_cvt_pk_bf16_f32 v209, v210, v211\n"
    "v_pk_mul_f32 v[212:213], v[212:213], v[70:71]\n"
    "v_pk_mul_f32 v[214:215], v[214:215], v[70:71]\n"
    "v_cvt_pk_bf16_f32 v212, v212, v213\n"
    "v_cvt_pk_bf16_f32 v213, v214, v215\n"
    "s_mov_b32 s53, 163840\n"
    "s_waitcnt lgkmcnt(5)\n"
    "buffer_store_dwordx4 v[40:43], v253, s[48:51], s53 offen nt\n"
    "ds_write_b64 v251, v[208:209] offset:128\n"
    "ds_write_b64 v251, v[212:213] offset:160\n"
    "v_pk_mul_f32 v[216:217], v[216:217], v[70:71]\n"
    "v_pk_mul_f32 v[218:219], v[218:219], v[70:71]\n"
    "v_cvt_pk_bf16_f32 v216, v216, v217\n"
    "v_cvt_pk_bf16_f32 v217, v218, v219\n"
    "v_pk_mul_f32 v[220:221], v[220:221], v[70:71]\n"
    "v_pk_mul_f32 v[222:223], v[222:223], v[70:71]\n"
    "v_cvt_pk_bf16_f32 v220, v220, v221\n"
    "v_cvt_pk_bf16_f32 v221, v222, v223\n"
    "s_mov_b32 s53, 180224\n"
    "s_waitcnt lgkmcnt(6)\n"
    "buffer_store_dwordx4 v[44:47], v253, s[48:51], s53 offen nt\n"
    "ds_write_b64 v251, v[216:217] offset:192\n"
    "ds_write_b64 v251, v[220:221] offset:224\n"
    "ds_read_b128 v[48:51], v252 offset:0\n"
    "ds_read_b128 v[52:55], v252 offset:1088\n"
    "ds_read_b128 v[56:59], v252 offset:2176\n"
    "ds_read_b128 v[60:63], v252 offset:3264\n"
    "s_mov_b32 s53, 196608\n"
    "s_waitcnt lgkmcnt(3)\n"
    "buffer_store_dwordx4 v[48:51], v253, s[48:51], s53 offen nt\n"
    "s_mov_b32 s53, 212992\n"
    "s_waitcnt lgkmcnt(2)\n"
    "buffer_store_dwordx4 v[52:55], v253, s[48:51], s53 offen nt\n"
    "s_mov_b32 s53, 229376\n"
    "s_waitcnt lgkmcnt(1)\n"
    "buffer_store_dwordx4 v[56:59], v253, s[48:51], s53 offen nt\n"
    "s_mov_b32 s53, 245760\n"
    "s_waitcnt lgkmcnt(0)\n"
    "buffer_store_dwordx4 v[60:63], v253, s[48:51], s53 offen nt\n"
    "s_or_b32 %0, s76, s77\n"
    "s_branch L4_end_%=\n"
    "L4_m0_0x_%=:\n"
    "s_nop 7\n"
    "v_cmp_ge_i32 s[68:69], 0, v250\n"
    "v_cmp_ge_i32 s[70:71], 1, v250\n"
    "v_cmp_ge_i32 s[72:73], 2, v250\n"
    "v_cmp_ge_i32 s[74:75], 3, v250\n"
    "s_nop 1\n"
    "v_cndmask_b32 v0, v0, v249, s[68:69]\n"
    "v_cndmask_b32 v1, v1, v249, s[70:71]\n"
    "v_cndmask_b32 v2, v2, v249, s[72:73]\n"
    "v_cndmask_b32 v3, v3, v249, s[74:75]\n"
    "v_cmp_ge_i32 s[68:69], 16, v250\n"
    "v_cmp_ge_i32 s[70:71], 17, v250\n"
    "v_cmp_ge_i32 s[72:73], 18, v250\n"
    "v_cmp_ge_i32 s[74:75], 19, v250\n"
    "s_nop 1\n"
    "v_cndmask_b32 v4, v4, v249, s[68:69]\n"
    "v_cndmask_b32 v5, v5, v249, s[70:71]\n"
    "v_cndmask_b32 v6, v6, v249, s[72:73]\n"
    "v_cndmask_b32 v7, v7, v249, s[74:75]\n"
    "s_branch L4_m0_0_%=\n"
    "L4_m1_0x_%=:\n"
    "v_cmp_ge_i32 s[68:69], 0, v248\n"
    "v_cmp_ge_i32 s[70:71], 1, v248\n"
    "v_cmp_ge_i32 s[72:73], 2, v248\n"
    "v_cmp_ge_i32 s[74:75], 3, v248\n"
    "s_nop 1\n"
    "v_cndmask_b32 v64, v64, v249, s[68:69]\n"
    "v_cndmask_b32 v65, v65, v249, s[70:71]\n"
    "v_cndmask_b32 v66, v66, v249, s[72:73]\n"
    "v_cndmask_b32 v67, v67, v249, s[74:75]\n"
    "v_cmp_ge_i32 s[68:69], 16, v248\n"
    "v_cmp_ge_i32 s[70:71], 17, v248\n"
    "v_cmp_ge_i32 s[72:73], 18, v248\n"
    "v_cmp_ge_i32 s[74:75], 19, v248\n"
    "s_nop 1\n"
    "v_cndmask_b32 v68, v68, v249, s[68:69]\n"
    "v_cndmask_b32 v69, v69, v249, s[70:71]\n"
    "v_cndmask_b32 v70, v70, v249, s[72:73]\n"
    "v_cndmask_b32 v71, v71, v249, s[74:75]\n"
    "s_branch L4_m1_0_%=\n"
    "L4_m0_1x_%=:\n"
    "s_nop 7\n"
    "v_cmp_ge_i32 s[68:69], 0, v250\n"
    "v_cmp_ge_i32 s[70:71], 1, v250\n"
    "v_cmp_ge_i32 s[72:73], 2, v250\n"
    "v_cmp_ge_i32 s[74:75], 3, v250\n"
    "s_nop 1\n"
    "v_cndmask_b32 v8, v8, v249, s[68:69]\n"
    "v_cndmask_b32 v9, v9, v249, s[70:71]\n"
    "v_cndmask_b32 v10, v10, v249, s[72:73]\n"
    "v_cndmask_b32 v11, v11, v249, s[74:75]\n"
    "v_cmp_ge_i32 s[68:69], 16, v250\n"
    "v_cmp_ge_i32 s[70:71], 17, v250\n"
    "v_cmp_ge_i32 s[72:73], 18, v250\n"
    "v_cmp_ge_i32 s[74:75], 19, v250\n"
    "s_nop 1\n"
    "v_cndmask_b32 v12, v12, v249, s[68:69]\n"
    "v_cndmask_b32 v13, v13, v249, s[70:71]\n"
    "v_cndmask_b32 v14, v14, v249, s[72:73]\n"
    "v_cndmask_b32 v15, v15, v249, s[74:75]\n"
    "s_branch L4_m0_1_%=\n"
    "L4_m1_1x_%=:\n"
    "v_cmp_ge_i32 s[68:69], 0, v248\n"
    "v_cmp_ge_i32 s[70:71], 1, v248\n"
    "v_cmp_ge_i32 s[72:73], 2, v248\n"
    "v_cmp_ge_i32 s[74:75], 3, v248\n"
    "s_nop 1\n"
    "v_cndmask_b32 v72, v72, v249, s[68:69]\n"
    "v_cndmask_b32 v73, v73, v249, s[70:71]\n"
    "v_cndmask_b32 v74, v74, v249, s[72:73]\n"
    "v_cndmask_b32 v75, v75, v249, s[74:75]\n"
    "v_cmp_ge_i32 s[68:69], 16, v248\n"
    "v_cmp_ge_i32 s[70:71], 17, v248\n"
    "v_cmp_ge_i32 s[72:73], 18, v248\n"
    "v_cmp_ge_i32 s[74:75], 19, v248\n"
    "s_nop 1\n"
    "v_cndmask_b32 v76, v76, v249, s[68:69]\n"
    "v_cndmask_b32 v77, v77, v249, s[70:71]\n"
    "v_cndmask_b32 v78, v78, v249, s[72:73]\n"
    "v_cndmask_b32 v79, v79, v249, s[74:75]\n"
    "s_branch L4_m1_1_%=\n"
    "L4_m0_2x_%=:\n"
    "s_nop 7\n"
    "v_cmp_ge_i32 s[68:69], 0, v250\n"
    "v_cmp_ge_i32 s[70:71], 1, v250\n"
    "v_cmp_ge_i32 s[72:73], 2, v250\n"
    "v_cmp_ge_i32 s[74:75], 3, v250\n"
    "s_nop 1\n"
    "v_cndmask_b32 v16, v16, v249, s[68:69]\n"
    "v_cndmask_b32 v17, v17, v249, s[70:71]\n"
    "v_cndmask_b32 v18, v18, v249, s[72:73]\n"
    "v_cndmask_b32 v19, v19, v249, s[74:75]\n"
    "v_cmp_ge_i32 s[68:69], 16, v250\n"
    "v_cmp_ge_i32 s[70:71], 17, v250\n"
    "v_cmp_ge_i32 s[72:73], 18, v250\n"
    "v_cmp_ge_i32 s[74:75], 19, v250\n"
    "s_nop 1\n"
    "v_cndmask_b32 v20, v20, v249, s[68:69]\n"
    "v_cndmask_b32 v21, v21, v249, s[70:71]\n"
    "v_cndmask_b32 v22, v22, v249, s[72:73]\n"
    "v_cndmask_b32 v23, v23, v249, s[74:75]\n"
    "s_branch L4_m0_2_%=\n"
    "L4_m1_2x_%=:\n"
    "v_cmp_ge_i32 s[68:69], 0, v248\n"
    "v_cmp_ge_i32 s[70:71], 1, v248\n"
    "v_cmp_ge_i32 s[72:73], 2, v248\n"
    "v_cmp_ge_i32 s[74:75], 3, v248\n"
    "s_nop 1\n"
    "v_cndmask_b32 v80, v80, v249, s[68:69]\n"
    "v_cndmask_b32 v81, v81, v249, s[70:71]\n"
    "v_cndmask_b32 v82, v82, v249, s[72:73]\n"
    "v_cndmask_b32 v83, v83, v249, s[74:75]\n"
    "v_cmp_ge_i32 s[68:69], 16, v248\n"
    "v_cmp_ge_i32 s[70:71], 17, v248\n"
    "v_cmp_ge_i32 s[72:73], 18, v248\n"
    "v_cmp_ge_i32 s[74:75], 19, v248\n"
    "s_nop 1\n"
    "v_cndmask_b32 v84, v84, v249, s[68:69]\n"
    "v_cndmask_b32 v85, v85, v249, s[70:71]\n"
    "v_cndmask_b32 v86, v86, v249, s[72:73]\n"
    "v_cndmask_b32 v87, v87, v249, s[74:75]\n"
    "s_branch L4_m1_2_%=\n"
    "L4_m0_3x_%=:\n"
    "s_nop 7\n"
    "v_cmp_ge_i32 s[68:69], 0, v250\n"
    "v_cmp_ge_i32 s[70:71], 1, v250\n"
    "v_cmp_ge_i32 s[72:73], 2, v250\n"
    "v_cmp_ge_i32 s[74:75], 3, v250\n"
    "s_nop 1\n"
    "v_cndmask_b32 v24, v24, v249, s[68:69]\n"
    "v_cndmask_b32 v25, v25, v249, s[70:71]\n"
    "v_cndmask_b32 v26, v26, v249, s[72:73]\n"
    "v_cndmask_b32 v27, v27, v249, s[74:75]\n"
    "v_cmp_ge_i32 s[68:69], 16, v250\n"
    "v_cmp_ge_i32 s[70:71], 17, v250\n"
    "v_cmp_ge_i32 s[72:73], 18, v250\n"
    "v_cmp_ge_i32 s[74:75], 19, v250\n"
    "s_nop 1\n"
    "v_cndmask_b32 v28, v28, v249, s[68:69]\n"
    "v_cndmask_b32 v29, v29, v249, s[70:71]\n"
    "v_cndmask_b32 v30, v30, v249, s[72:73]\n"
    "v_cndmask_b32 v31, v31, v249, s[74:75]\n"
    "s_branch L4_m0_3_%=\n"
    "L4_m1_3x_%=:\n"
    "v_cmp_ge_i32 s[68:69], 0, v248\n"
    "v_cmp_ge_i32 s[70:71], 1, v248\n"
    "v_cmp_ge_i32 s[72:73], 2, v248\n"
    "v_cmp_ge_i32 s[74:75], 3, v248\n"
    "s_nop 1\n"
    "v_cndmask_b32 v88, v88, v249, s[68:69]\n"
    "v_cndmask_b32 v89, v89, v249, s[70:71]\n"
    "v_cndmask_b32 v90, v90, v249, s[72:73]\n"
    "v_cndmask_b32 v91, v91, v249, s[74:75]\n"
    "v_cmp_ge_i32 s[68:69], 16, v248\n"
    "v_cmp_ge_i32 s[70:71], 17, v248\n"
    "v_cmp_ge_i32 s[72:73], 18, v248\n"
    "v_cmp_ge_i32 s[74:75], 19, v248\n"
    "s_nop 1\n"
    "v_cndmask_b32 v92, v92, v249, s[68:69]\n"
    "v_cndmask_b32 v93, v93, v249, s[70:71]\n"
    "v_cndmask_b32 v94, v94, v249, s[72:73]\n"
    "v_cndmask_b32 v95, v95, v249, s[74:75]\n"
    "s_branch L4_m1_3_%=\n"
    "L4_fix0_%=:\n"
    "v_cmp_ge_i32 s[68:69], 0, v248\n"
    "v_cmp_ge_i32 s[70:71], 1, v248\n"
    "v_cmp_ge_i32 s[72:73], 2, v248\n"
    "v_cmp_ge_i32 s[74:75], 3, v248\n"
    "s_nop 1\n"
    "v_cndmask_b32 v64, v64, v249, s[68:69]\n"
    "v_cndmask_b32 v72, v72, v249, s[68:69]\n"
    "v_cndmask_b32 v80, v80, v249, s[68:69]\n"
    "v_cndmask_b32 v88, v88, v249, s[68:69]\n"
    "v_cndmask_b32 v65, v65, v249, s[70:71]\n"
    "v_cndmask_b32 v73, v73, v249, s[70:71]\n"
    "v_cndmask_b32 v81, v81, v249, s[70:71]\n"
    "v_cndmask_b32 v89, v89, v249, s[70:71]\n"
    "v_cndmask_b32 v66, v66, v249, s[72:73]\n"
    "v_cndmask_b32 v74, v74, v249, s[72:73]\n"
    "v_cndmask_b32 v82, v82, v249, s[72:73]\n"
    "v_cndmask_b32 v90, v90, v249, s[72:73]\n"
    "v_cndmask_b32 v67, v67, v249, s[74:75]\n"
    "v_cndmask_b32 v75, v75, v249, s[74:75]\n"
    "v_cndmask_b32 v83, v83, v249, s[74:75]\n"
    "v_cndmask_b32 v91, v91, v249, s[74:75]\n"
    "v_cmp_ge_i32 s[68:69], 16, v248\n"
    "v_cmp_ge_i32 s[70:71], 17, v248\n"
    "v_cmp_ge_i32 s[72:73], 18, v248\n"
    "v_cmp_ge_i32 s[74:75], 19, v248\n"
    "s_nop 1\n"
    "v_cndmask_b32 v68, v68, v249, s[68:69]\n"
    "v_cndmask_b32 v76, v76, v249, s[68:69]\n"
    "v_cndmask_b32 v84, v84, v249, s[68:69]\n"
    "v_cndmask_b32 v92, v92, v249, s[68:69]\n"
    "v_cndmask_b32 v69, v69, v249, s[70:71]\n"
    "v_cndmask_b32 v77, v77, v249, s[70:71]\n"
    "v_cndmask_b32 v85, v85, v249, s[70:71]\n"
    "v_cndmask_b32 v93, v93, v249, s[70:71]\n"
    "v_cndmask_b32 v70, v70, v249, s[72:73]\n"
    "v_cndmask_b32 v78, v78, v249, s[72:73]\n"
    "v_cndmask_b32 v86, v86, v249, s[72:73]\n"
    "v_cndmask_b32 v94, v94, v249, s[72:73]\n"
    "v_cndmask_b32 v71, v71, v249, s[74:75]\n"
    "v_cndmask_b32 v79, v79, v249, s[74:75]\n"
    "v_cndmask_b32 v87, v87, v249, s[74:75]\n"
    "v_cndmask_b32 v95, v95, v249, s[74:75]\n"
    "s_mov_b32 s57, 0\n"
    "s_mov_b32 s56, 0\n"
    "s_branch L4_p1_%=\n"
    "L4_fix1_%=:\n"
    "v_cmp_ge_i32 s[68:69], 0, v248\n"
    "v_cmp_ge_i32 s[70:71], 1, v248\n"
    "v_cmp_ge_i32 s[72:73], 2, v248\n"
    "v_cmp_ge_i32 s[74:75], 3, v248\n"
    "s_nop 1\n"
    "v_cndmask_b32 v64, v64, v249, s[68:69]\n"
    "v_cndmask_b32 v72, v72, v249, s[68:69]\n"
    "v_cndmask_b32 v80, v80, v249, s[68:69]\n"
    "v_cndmask_b32 v88, v88, v249, s[68:69]\n"
    "v_cndmask_b32 v65, v65, v249, s[70:71]\n"
    "v_cndmask_b32 v73, v73, v249, s[70:71]\n"
    "v_cndmask_b32 v81, v81, v249, s[70:71]\n"
    "v_cndmask_b32 v89, v89, v249, s[70:71]\n"
    "v_cndmask_b32 v66, v66, v249, s[72:73]\n"
    "v_cndmask_b32 v74, v74, v249, s[72:73]\n"
    "v_cndmask_b32 v82, v82, v249, s[72:73]\n"
    "v_cndmask_b32 v90, v90, v249, s[72:73]\n"
    "v_cndmask_b32 v67, v67, v249, s[74:75]\n"
    "v_cndmask_b32 v75, v75, v249, s[74:75]\n"
    "v_cndmask_b32 v83, v83, v249, s[74:75]\n"
    "v_cndmask_b32 v91, v91, v249, s[74:75]\n"
    "v_cmp_ge_i32 s[68:69], 16, v248\n"
    "v_cmp_ge_i32 s[70:71], 17, v248\n"
    "v_cmp_ge_i32 s[72:73], 18, v248\n"
    "v_cmp_ge_i32 s[74:75], 19, v248\n"
    "s_nop 1\n"
    "v_cndmask_b32 v68, v68, v249, s[68:69]\n"
    "v_cndmask_b32 v76, v76, v249, s[68:69]\n"
    "v_cndmask_b32 v84, v84, v249, s[68:69]\n"
    "v_cndmask_b32 v92, v92, v249, s[68:69]\n"
    "v_cndmask_b32 v69, v69, v249, s[70:71]\n"
    "v_cndmask_b32 v77, v77, v249, s[70:71]\n"
    "v_cndmask_b32 v85, v85, v249, s[70:71]\n"
    "v_cndmask_b32 v93, v93, v249, s[70:71]\n"
    "v_cndmask_b32 v70, v70, v249, s[72:73]\n"
    "v_cndmask_b32 v78, v78, v249, s[72:73]\n"
    "v_cndmask_b32 v86, v86, v249, s[72:73]\n"
    "v_cndmask_b32 v94, v94, v249, s[72:73]\n"
    "v_cndmask_b32 v71, v71, v249, s[74:75]\n"
    "v_cndmask_b32 v79, v79, v249, s[74:75]\n"
    "v_cndmask_b32 v87, v87, v249, s[74:75]\n"
    "v_cndmask_b32 v95, v95, v249, s[74:75]\n"
    "s_mov_b32 s57, 0\n"
    "s_mov_b32 s56, 0\n"
    "s_branch L4_p0_%=\n"
    "L4_ev0_%=:\n"
    "s_cmp_eq_u32 s57, 0\n"
    "s_cbranch_scc0 L4_fix0_%=\n"
    "v_mfma_f32_16x16x32_bf16 v[96:99], a[96:99], v[4:7], v[96:99]\n"
    "s_mov_b32 s61, 0x5f800000\n"
    "ds_read_b64_tr_b16 a[64:65], v225 offset:8192\n"
    "ds_read_b64_tr_b16 a[66:67], v225 offset:12288\n"
    "v_mfma_f32_16x16x32_bf16 v[100:103], a[100:103], v[4:7], v[100:103]\n"
    "s_mov_b64 s[76:77], 0\n"
    "v_exp_f32 v38, v38\n"
    "v_mfma_f32_16x16x32_bf16 v[104:107], a[104:107], v[4:7], v[104:107]\n"
    "ds_read_b64_tr_b16 a[68:69], v225 offset:8448\n"
    "ds_read_b64_tr_b16 a[70:71], v225 offset:12544\n"
    "v_mfma_f32_16x16x32_bf16 v[108:111], a[108:111], v[4:7], v[108:111]\n"
    "v_exp_f32 v39, v39\n"
    "v_mfma_f32_16x16x32_bf16 v[112:115], a[112:115], v[4:7], v[112:115]\n"
    "ds_read_b64_tr_b16 a[72:73], v225 offset:8704\n"
    "ds_read_b64_tr_b16 a[74:75], v225 offset:12800\n"
    "v_mfma_f32_16x16x32_bf16 v[116:119], a[116:119], v[4:7], v[116:119]\n"
    "v_exp_f32 v36, v36\n"
    "v_mfma_f32_16x16x32_bf16 v[120:123], a[120:123], v[4:7], v[120:123]\n"
    "ds_read_b64_tr_b16 a[76:77], v225 offset:8960\n"
    "ds_read_b64_tr_b16 a[78:79], v225 offset:13056\n"
    "v_mfma_f32_16x16x32_bf16 v[124:127], a[124:127], v[4:7], v[124:127]\n"
    "v_exp_f32 v37, v37\n"
    "v_mfma_f32_16x16x32_bf16 a[128:131], a[144:147], v[4:7], a[128:131]\n"
    "ds_read_b64_tr_b16 a[80:81], v225 offset:9216\n"
    "ds_read_b64_tr_b16 a[82:83], v225 offset:13312\n"
    "v_mfma_f32_16x16x32_bf16 v[128:131], a[96:99], v[8:11], v[128:131]\n"
    "v_cvt_pk_bf16_f32 v39, v38, v39\n"
    "v_cvt_pk_bf16_f32 v38, v36, v37\n"
    "v_mfma_f32_16x16x32_bf16 v[132:135], a[100:103], v[8:11], v[132:135]\n"
    "ds_read_b64_tr_b16 a[84:85], v225 offset:9472\n"
    "ds_read_b64_tr_b16 a[86:87], v225 offset:13568\n"
    "v_mfma_f32_16x16x32_bf16 v[136:139], a[104:107], v[8:11], v[136:139]\n"
    "v_exp_f32 v34, v34\n"
    "v_mfma_f32_16x16x32_bf16 v[140:143], a[108:111], v[8:11], v[140:143]\n"
    "ds_read_b64_tr_b16 a[88:89], v225 offset:9728\n"
    "ds_read_b64_tr_b16 a[90:91], v225 offset:13824\n"
    "v_mfma_f32_16x16x32_bf16 v[144:147], a[112:115], v[8:11], v[144:147]\n"
    "v_exp_f32 v35, v35\n"
    "v_mfma_f32_16x16x32_bf16 v[148:151], a[116:119], v[8:11], v[148:151]\n"
    "ds_read_b64_tr_b16 a[92:93], v225 offset:9984\n"
    "ds_read_b64_tr_b16 a[94:95], v225 offset:14080\n"
    "v_mfma_f32_16x16x32_bf16 v[152:155], a[120:123], v[8:11], v[152:155]\n"
    "v_exp_f32 v32, v32\n"
    "v_mfma_f32_16x16x32_bf16 v[156:159], a[124:127], v[8:11], v[156:159]\n"
    "v_exp_f32 v33, v33\n"
    "v_mfma_f32_16x16x32_bf16 a[132:135], a[144:147], v[8:11], a[132:135]\n"
    "v_cvt_pk_bf16_f32 v37, v34, v35\n"
    "v_cvt_pk_bf16_f32 v36, v32, v33\n"
    "v_mfma_f32_16x16x32_bf16 v[160:163], a[96:99], v[20:23], v[160:163]\n"
    "v_exp_f32 v40, v40\n"
    "v_mfma_f32_16x16x32_bf16 v[164:167], a[100:103], v[20:23], v[164:167]\n"
    "v_exp_f32 v41, v41\n"
    "v_mfma_f32_16x16x32_bf16 v[168:171], a[104:107], v[20:23], v[168:171]\n"
    "v_exp_f32 v42, v42\n"
    "v_mfma_f32_16x16x32_bf16 v[172:175], a[108:111], v[20:23], v[172:175]\n"
    "v_exp_f32 v43, v43\n"
    "v_mfma_f32_16x16x32_bf16 v[176:179], a[112:115], v[20:23], v[176:179]\n"
    "v_cvt_pk_bf16_f32 v40, v40, v41\n"
    "v_cvt_pk_bf16_f32 v41, v42, v43\n"
    "v_mfma_f32_16x16x32_bf16 v[180:183], a[116:119], v[20:23], v[180:183]\n"
    "v_exp_f32 v44, v44\n"
    "v_mfma_f32_16x16x32_bf16 v[184:187], a[120:123], v[20:23], v[184:187]\n"
    "v_exp_f32 v45, v45\n"
    "v_mfma_f32_16x16x32_bf16 v[188:191], a[124:127], v[20:23], v[188:191]\n"
    "v_exp_f32 v46, v46\n"
    "v_mfma_f32_16x16x32_bf16 a[136:139], a[144:147], v[20:23], a[136:139]\n"
    "v_exp_f32 v47, v47\n"
    "v_mfma_f32_16x16x32_bf16 v[192:195], a[96:99], v[24:27], v[192:195]\n"
    "v_cvt_pk_bf16_f32 v42, v44, v45\n"
    "v_cvt_pk_bf16_f32 v43, v46, v47\n"
    "v_mfma_f32_16x16x32_bf16 v[196:199], a[100:103], v[24:27], v[196:199]\n"
    "v_exp_f32 v54, v54\n"
    "v_mfma_f32_16x16x32_bf16 v[200:203], a[104:107], v[24:27], v[200:203]\n"
    "v_exp_f32 v55, v55\n"
    "v_mfma_f32_16x16x32_bf16 v[204:207], a[108:111], v[24:27], v[204:207]\n"
    "v_exp_f32 v52, v52\n"
    "v_mfma_f32_16x16x32_bf16 v[208:211], a[112:115], v[24:27], v[208:211]\n"
    "v_exp_f32 v53, v53\n"
    "v_mfma_f32_16x16x32_bf16 v[212:215], a[116:119], v[24:27], v[212:215]\n"
    "v_cvt_pk_bf16_f32 v55, v54, v55\n"
    "v_cvt_pk_bf16_f32 v54, v52, v53\n"
    "v_mfma_f32_16x16x32_bf16 v[216:219], a[120:123], v[24:27], v[216:219]\n"
    "v_exp_f32 v50, v50\n"
    "v_mfma_f32_16x16x32_bf16 v[220:223], a[124:127], v[24:27], v[220:223]\n"
    "v_exp_f32 v51, v51\n"
    "v_mfma_f32_16x16x32_bf16 a[140:143], a[144:147], v[24:27], a[140:143]\n"
    "v_exp_f32 v48, v48\n"
    "s_waitcnt lgkmcnt(0)\n"
    "v_mfma_f32_16x16x32_bf16 a[128:131], a[144:147], v[36:39], a[128:131]\n"
    "v_exp_f32 v49, v49\n"
    "v_mfma_f32_16x16x32_bf16 v[96:99], a[64:67], v[36:39], v[96:99]\n"
    "v_cvt_pk_bf16_f32 v53, v50, v51\n"
    "v_cvt_pk_bf16_f32 v52, v48, v49\n"
    "v_mfma_f32_16x16x32_bf16 v[100:103], a[68:71], v[36:39], v[100:103]\n"
    "v_exp_f32 v56, v56\n"
    "v_mfma_f32_16x16x32_bf16 v[104:107], a[72:75], v[36:39], v[104:107]\n"
    "v_exp_f32 v57, v57\n"
    "v_mfma_f32_16x16x32_bf16 v[108:111], a[76:79], v[36:39], v[108:111]\n"
    "v_exp_f32 v58, v58\n"
    "v_mfma_f32_16x16x32_bf16 v[112:115], a[80:83], v[36:39], v[112:115]\n"
    "v_exp_f32 v59, v59\n"
    "v_mfma_f32_16x16x32_bf16 v[116:119], a[84:87], v[36:39], v[116:119]\n"
    "v_cvt_pk_bf16_f32 v56, v56, v57\n"
    "v_cvt_pk_bf16_f32 v57, v58, v59\n"
    "v_mfma_f32_16x16x32_bf16 v[120:123], a[88:91], v[36:39], v[120:123]\n"
    "v_exp_f32 v60, v60\n"
    "v_mfma_f32_16x16x32_bf16 v[124:127], a[92:95], v[36:39], v[124:127]\n"
    "v_exp_f32 v61, v61\n"
    "v_mfma_f32_16x16x32_bf16 a[132:135], a[144:147], v[40:43], a[132:135]\n"
    "v_exp_f32 v62, v62\n"
    "v_mfma_f32_16x16x32_bf16 v[128:131], a[64:67], v[40:43], v[128:131]\n"
    "v_exp_f32 v63, v63\n"
    "v_mfma_f32_16x16x32_bf16 v[132:135], a[68:71], v[40:43], v[132:135]\n"
    "v_cvt_pk_bf16_f32 v58, v60, v61\n"
    "v_cvt_pk_bf16_f32 v59, v62, v63\n"
    "v_mfma_f32_16x16x32_bf16 v[136:139], a[72:75], v[40:43], v[136:139]\n"
    "v_accvgpr_read_b32 v88, a128\n"
    "v_accvgpr_read_b32 v64, a152\n"
    "v_mfma_f32_16x16x32_bf16 v[140:143], a[76:79], v[40:43], v[140:143]\n"
    "v_accvgpr_read_b32 v65, a153\n"
    "v_add_f32 v88, v88, v64\n"
    "v_mfma_f32_16x16x32_bf16 v[144:147], a[80:83], v[40:43], v[144:147]\n"
    "v_add_f32 v88, v88, v65\n"
    "v_cmp_nge_f32 vcc, s61, v88\n"
    "v_mfma_f32_16x16x32_bf16 v[148:151], a[84:87], v[40:43], v[148:151]\n"
    "v_rcp_f32 v64, v88\n"
    "v_mfma_f32_16x16x32_bf16 v[152:155], a[88:91], v[40:43], v[152:155]\n"
    "s_or_b64 s[76:77], s[76:77], vcc\n"
    "v_mov_b32 v65, v64\n"
    "v_mfma_f32_16x16x32_bf16 v[156:159], a[92:95], v[40:43], v[156:159]\n"
    "v_mul_f32 v96, v96, v64\n"
    "v_mul_f32 v97, v97, v64\n"
    "v_mfma_f32_16x16x32_bf16 a[136:139], a[144:147], v[52:55], a[136:139]\n"
    "v_mul_f32 v98, v98, v64\n"
    "v_mul_f32 v99, v99, v64\n"
    "v_mfma_f32_16x16x32_bf16 v[160:163], a[64:67], v[52:55], v[160:163]\n"
    "v_cvt_pk_bf16_f32 v96, v96, v97\n"
    "v_cvt_pk_bf16_f32 v97, v98, v99\n"
    "v_mfma_f32_16x16x32_bf16 v[164:167], a[68:71], v[52:55], v[164:167]\n"
    "ds_write_b64 v251, v[96:97] offset:0\n"
    "v_mul_f32 v100, v100, v64\n"
    "v_mfma_f32_16x16x32_bf16 v[168:171], a[72:75], v[52:55], v[168:171]\n"
    "v_mul_f32 v101, v101, v64\n"
    "v_mul_f32 v102, v102, v64\n"
    "v_mfma_f32_16x16x32_bf16 v[172:175], a[76:79], v[52:55], v[172:175]\n"
    "v_mul_f32 v103, v103, v64\n"
    "v_cvt_pk_bf16_f32 v100, v100, v101\n"
    "v_mfma_f32_16x16x32_bf16 v[176:179], a[80:83], v[52:55], v[176:179]\n"
    "v_cvt_pk_bf16_f32 v101, v102, v103\n"
    "v_mul_f32 v104, v104, v64\n"
    "v_mfma_f32_16x16x32_bf16 v[180:183], a[84:87], v[52:55], v[180:183]\n"
    "ds_write_b64 v251, v[100:101] offset:32\n"
    "v_mul_f32 v105, v105, v64\n"
    "v_mfma_f32_16x16x32_bf16 v[184:187], a[88:91], v[52:55], v[184:187]\n"
    "v_mul_f32 v106, v106, v64\n"
    "v_mul_f32 v107, v107, v64\n"
    "v_mfma_f32_16x16x32_bf16 v[188:191], a[92:95], v[52:55], v[188:191]\n"
    "v_cvt_pk_bf16_f32 v104, v104, v105\n"
    "v_cvt_pk_bf16_f32 v105, v106, v107\n"
    "v_mfma_f32_16x16x32_bf16 a[140:143], a[144:147], v[56:59], a[140:143]\n"
    "ds_write_b64 v251, v[104:105] offset:64\n"
    "v_mul_f32 v108, v108, v64\n"
    "v_mfma_f32_16x16x32_bf16 v[192:195], a[64:67], v[56:59], v[192:195]\n"
    "v_mul_f32 v109, v109, v64\n"
    "v_mul_f32 v110, v110, v64\n"
    "v_mfma_f32_16x16x32_bf16 v[196:199], a[68:71], v[56:59], v[196:199]\n"
    "v_mul_f32 v111, v111, v64\n"
    "v_cvt_pk_bf16_f32 v108, v108, v109\n"
    "v_mfma_f32_16x16x32_bf16 v[200:203], a[72:75], v[56:59], v[200:203]\n"
    "v_cvt_pk_bf16_f32 v109, v110, v111\n"
    "v_mul_f32 v112, v112, v64\n"
    "v_mfma_f32_16x16x32_bf16 v[204:207], a[76:79], v[56:59], v[204:207]\n"
    "ds_write_b64 v251, v[108:109] offset:96\n"
    "v_mul_f32 v113, v113, v64\n"
    "v_mfma_f32_16x16x32_bf16 v[208:211], a[80:83], v[56:59], v[208:211]\n"
    "v_mul_f32 v114, v114, v64\n"
    "v_mul_f32 v115, v115, v64\n"
    "v_mfma_f32_16x16x32_bf16 v[212:215], a[84:87], v[56:59], v[212:215]\n"
    "v_cvt_pk_bf16_f32 v112, v112, v113\n"
    "v_cvt_pk_bf16_f32 v113, v114, v115\n"
    "v_mfma_f32_16x16x32_bf16 v[216:219], a[88:91], v[56:59], v[216:219]\n"
    "ds_write_b64 v251, v[112:113] offset:128\n"
    "v_mul_f32 v116, v116, v64\n"
    "v_mfma_f32_16x16x32_bf16 v[220:223], a[92:95], v[56:59], v[220:223]\n"
    "v_mul_f32 v117, v117, v64\n"
    "v_mul_f32 v118, v118, v64\n"
    "v_mul_f32 v119, v119, v64\n"
    "v_cvt_pk_bf16_f32 v116, v116, v117\n"
    "v_cvt_pk_bf16_f32 v117, v118, v119\n"
    "v_pk_mul_f32 v[120:121], v[120:121], v[64:65]\n"
    "v_pk_mul_f32 v[122:123], v[122:123], v[64:65]\n"
    "v_cvt_pk_bf16_f32 v120, v120, v121\n"
    "v_cvt_pk_bf16_f32 v121, v122, v123\n"
    "v_pk_mul_f32 v[124:125], v[124:125], v[64:65]\n"
    "v_pk_mul_f32 v[126:127], v[126:127], v[64:65]\n"
    "v_cvt_pk_bf16_f32 v124, v124, v125\n"
    "v_cvt_pk_bf16_f32 v125, v126, v127\n"
    "ds_write_b64 v251, v[116:117] offset:160\n"
    "ds_write_b64 v251, v[120:121] offset:192\n"
    "ds_write_b64 v251, v[124:125] offset:224\n"
    "ds_read_b128 v[0:3], v252 offset:0\n"
    "ds_read_b128 v[4:7], v252 offset:1088\n"
    "ds_read_b128 v[8:11], v252 offset:2176\n"
    "ds_read_b128 v[12:15], v252 offset:3264\n"
    "v_accvgpr_read_b32 v89, a132\n"
    "v_accvgpr_read_b32 v66, a154\n"
    "v_accvgpr_read_b32 v67, a155\n"
    "v_add_f32 v89, v89, v66\n"
    "v_add_f32 v89, v89, v67\n"
    "v_cmp_nge_f32 vcc, s61, v89\n"
    "v_rcp_f32 v66, v89\n"
    "s_or_b64 s[76:77], s[76:77], vcc\n"
    "v_mov_b32 v67, v66\n"
    "v_pk_mul_f32 v[128:129], v[128:129], v[66:67]\n"
    "v_pk_mul_f32 v[130:131], v[130:131], v[66:67]\n"
    "v_cvt_pk_bf16_f32 v128, v128, v129\n"
    "v_cvt_pk_bf16_f32 v129, v130, v131\n"
    "v_pk_mul_f32 v[132:133], v[132:133], v[66:67]\n"
    "v_pk_mul_f32 v[134:135], v[134:135], v[66:67]\n"
    "v_cvt_pk_bf16_f32 v132, v132, v133\n"
    "v_cvt_pk_bf16_f32 v133, v134, v135\n"
    "s_mov_b32 s53, 0\n"
    "s_waitcnt lgkmcnt(3)\n"
    "buffer_store_dwordx4 v[0:3], v253, s[48:51], s53 offen nt\n"
    "ds_write_b64 v251, v[128:129] offset:4352\n"
    "ds_write_b64 v251, v[132:133] offset:4384\n"
    "v_pk_mul_f32 v[136:137], v[136:137], v[66:67]\n"
    "v_pk_mul_f32 v[138:139], v[138:139], v[66:67]\n"
    "v_cvt_pk_bf16_f32 v136, v136, v137\n"
    "v_cvt_pk_bf16_f32 v137, v138, v139\n"
    "v_pk_mul_f32 v[140:141], v[140:141], v[66:67]\n"
    "v_pk_mul_f32 v[142:143], v[142:143], v[66:67]\n"
    "v_cvt_pk_bf16_f32 v140, v140, v141\n"
    "v_cvt_pk_bf16_f32 v141, v142, v143\n"
    "s_mov_b32 s53, 16384\n"
    "s_waitcnt lgkmcnt(4)\n"
    "buffer_store_dwordx4 v[4:7], v253, s[48:51], s53 offen nt\n"
    "ds_write_b64 v251, v[136:137] offset:4416\n"
    "ds_write_b64 v251, v[140:141] offset:4448\n"
    "v_pk_mul_f32 v[144:145], v[144:145], v[66:67]\n"
    "v_pk_mul_f32 v[146:147], v[146:147], v[66:67]\n"
    "v_cvt_pk_bf16_f32 v144, v144, v145\n"
    "v_cvt_pk_bf16_f32 v145, v146, v147\n"
    "v_pk_mul_f32 v[148:149], v[148:149], v[66:67]\n"
    "v_pk_mul_f32 v[150:151], v[150:151], v[66:67]\n"
    "v_cvt_pk_bf16_f32 v148, v148, v149\n"
    "v_cvt_pk_bf16_f32 v149, v150, v151\n"
    "s_mov_b32 s53, 32768\n"
    "s_waitcnt lgkmcnt(5)\n"
    "buffer_store_dwordx4 v[8:11], v253, s[48:51], s53 offen nt\n"
    "ds_write_b64 v251, v[144:145] offset:4480\n"
    "ds_write_b64 v251, v[148:149] offset:4512\n"
    "v_pk_mul_f32 v[152:153], v[152:153], v[66:67]\n"
    "v_pk_mul_f32 v[154:155], v[154:155], v[66:67]\n"
    "v_cvt_pk_bf16_f32 v152, v152, v153\n"
    "v_cvt_pk_bf16_f32 v153, v154, v155\n"
    "v_pk_mul_f32 v[156:157], v[156:157], v[66:67]\n"
    "v_pk_mul_f32 v[158:159], v[158:159], v[66:67]\n"
    "v_cvt_pk_bf16_f32 v156, v156, v157\n"
    "v_cvt_pk_bf16_f32 v157, v158, v159\n"
    "s_mov_b32 s53, 49152\n"
    "s_waitcnt lgkmcnt(6)\n"
    "buffer_store_dwordx4 v[12:15], v253, s[48:51], s53 offen nt\n"
    "ds_write_b64 v251, v[152:153] offset:4544\n"
    "ds_write_b64 v251, v[156:157] offset:4576\n"
    "ds_read_b128 v[16:19], v252 offset:4352\n"
    "ds_read_b128 v[20:23], v252 offset:5440\n"
    "ds_read_b128 v[24:27], v252 offset:6528\n"
    "ds_read_b128 v[28:31], v252 offset:7616\n"
    "v_accvgpr_read_b32 v90, a136\n"
    "v_accvgpr_read_b32 v68, a156\n"
    "v_accvgpr_read_b32 v69, a157\n"
    "v_add_f32 v90, v90, v68\n"
    "v_add_f32 v90, v90, v69\n"
    "v_cmp_nge_f32 vcc, s61, v90\n"
    "v_rcp_f32 v68, v90\n"
    "s_or_b64 s[76:77], s[76:77], vcc\n"
    "v_mov_b32 v69, v68\n"
    "v_pk_mul_f32 v[160:161], v[160:161], v[68:69]\n"
    "v_pk_mul_f32 v[162:163], v[162:163], v[68:69]\n"
    "v_cvt_pk_bf16_f32 v160, v160, v161\n"
    "v_cvt_pk_bf16_f32 v161, v162, v163\n"
    "v_pk_mul_f32 v[164:165], v[164:165], v[68:69]\n"
    "v_pk_mul_f32 v[166:167], v[166:167], v[68:69]\n"
    "v_cvt_pk_bf16_f32 v164, v164, v165\n"
    "v_cvt_pk_bf16_f32 v165, v166, v167\n"
    "s_mov_b32 s53, 65536\n"
    "s_waitcnt lgkmcnt(3)\n"
    "buffer_store_dwordx4 v[16:19], v253, s[48:51], s53 offen nt\n"
    "ds_write_b64 v251, v[160:161] offset:8704\n"
    "ds_write_b64 v251, v[164:165] offset:8736\n"
    "v_pk_mul_f32 v[168:169], v[168:169], v[68:69]\n"
    "v_pk_mul_f32 v[170:171], v[170:171], v[68:69]\n"
    "v_cvt_pk_bf16_f32 v168, v168, v169\n"
    "v_cvt_pk_bf16_f32 v169, v170, v171\n"
    "v_pk_mul_f32 v[172:173], v[172:173], v[68:69]\n"
    "v_pk_mul_f32 v[174:175], v[174:175], v[68:69]\n"
    "v_cvt_pk_bf16_f32 v172, v172, v173\n"
    "v_cvt_pk_bf16_f32 v173, v174, v175\n"
    "s_mov_b32 s53, 81920\n"
    "s_waitcnt lgkmcnt(4)\n"
    "buffer_store_dwordx4 v[20:23], v253, s[48:51], s53 offen nt\n"
    "ds_write_b64 v251, v[168:169] offset:8768\n"
    "ds_write_b64 v251, v[172:173] offset:8800\n"
    "v_pk_mul_f32 v[176:177], v[176:177], v[68:69]\n"
    "v_pk_mul_f32 v[178:179], v[178:179], v[68:69]\n"
    "v_cvt_pk_bf16_f32 v176, v176, v177\n"
    "v_cvt_pk_bf16_f32 v177, v178, v179\n"
    "v_pk_mul_f32 v[180:181], v[180:181], v[68:69]\n"
    "v_pk_mul_f32 v[182:183], v[182:183], v[68:69]\n"
    "v_cvt_pk_bf16_f32 v180, v180, v181\n"
    "v_cvt_pk_bf16_f32 v181, v182, v183\n"
    "s_mov_b32 s53, 98304\n"
    "s_waitcnt lgkmcnt(5)\n"
    "buffer_store_dwordx4 v[24:27], v253, s[48:51], s53 offen nt\n"
    "ds_write_b64 v251, v[176:177] offset:8832\n"
    "ds_write_b64 v251, v[180:181] offset:8864\n"
    "v_pk_mul_f32 v[184:185], v[184:185], v[68:69]\n"
    "v_pk_mul_f32 v[186:187], v[186:187], v[68:69]\n"
    "v_cvt_pk_bf16_f32 v184, v184, v185\n"
    "v_cvt_pk_bf16_f32 v185, v186, v187\n"
    "v_pk_mul_f32 v[188:189], v[188:189], v[68:69]\n"
    "v_pk_mul_f32 v[190:191], v[190:191], v[68:69]\n"
    "v_cvt_pk_bf16_f32 v188, v188, v189\n"
    "v_cvt_pk_bf16_f32 v189, v190, v191\n"
    "s_mov_b32 s53, 114688\n"
    "s_waitcnt lgkmcnt(6)\n"
    "buffer_store_dwordx4 v[28:31], v253, s[48:51], s53 offen nt\n"
    "ds_write_b64 v251, v[184:185] offset:8896\n"
    "ds_write_b64 v251, v[188:189] offset:8928\n"
    "ds_read_b128 v[0:3], v252 offset:8704\n"
    "ds_read_b128 v[4:7], v252 offset:9792\n"
    "ds_read_b128 v[8:11], v252 offset:10880\n"
    "ds_read_b128 v[12:15], v252 offset:11968\n"
    "v_accvgpr_read_b32 v91, a140\n"
    "v_accvgpr_read_b32 v70, a158\n"
    "v_accvgpr_read_b32 v71, a159\n"
    "v_add_f32 v91, v91, v70\n"
    "v_add_f32 v91, v91, v71\n"
    "v_cmp_nge_f32 vcc, s61, v91\n"
    "v_rcp_f32 v70, v91\n"
    "s_or_b64 s[76:77], s[76:77], vcc\n"
    "v_mov_b32 v71, v70\n"
    "v_pk_mul_f32 v[192:193], v[192:193], v[70:71]\n"
    "v_pk_mul_f32 v[194:195], v[194:195], v[70:71]\n"
    "v_cvt_pk_bf16_f32 v192, v192, v193\n"
    "v_cvt_pk_bf16_f32 v193, v194, v195\n"
    "v_pk_mul_f32 v[196:197], v[196:197], v[70:71]\n"
    "v_pk_mul_f32 v[198:199], v[198:199], v[70:71]\n"
    "v_cvt_pk_bf16_f32 v196, v196, v197\n"
    "v_cvt_pk_bf16_f32 v197, v198, v199\n"
    "s_mov_b32 s53, 131072\n"
    "s_waitcnt lgkmcnt(3)\n"
    "buffer_store_dwordx4 v[0:3], v253, s[48:51], s53 offen nt\n"
    "ds_write_b64 v251, v[192:193] offset:0\n"
    "ds_write_b64 v251, v[196:197] offset:32\n"
    "v_pk_mul_f32 v[200:201], v[200:201], v[70:71]\n"
    "v_pk_mul_f32 v[202:203], v[202:203], v[70:71]\n"
    "v_cvt_pk_bf16_f32 v200, v200, v201\n"
    "v_cvt_pk_bf16_f32 v201, v202, v203\n"
    "v_pk_mul_f32 v[204:205], v[204:205], v[70:71]\n"
    "v_pk_mul_f32 v[206:207], v[206:207], v[70:71]\n"
    "v_cvt_pk_bf16_f32 v204, v204, v205\n"
    "v_cvt_pk_bf16_f32 v205, v206, v207\n"
    "s_mov_b32 s53, 147456\n"
    "s_waitcnt lgkmcnt(4)\n"
    "buffer_store_dwordx4 v[4:7], v253, s[48:51], s53 offen nt\n"
    "ds_write_b64 v251, v[200:201] offset:64\n"
    "ds_write_b64 v251, v[204:205] offset:96\n"
    "v_pk_mul_f32 v[208:209], v[208:209], v[70:71]\n"
    "v_pk_mul_f32 v[210:211], v[210:211], v[70:71]\n"
    "v_cvt_pk_bf16_f32 v208, v208, v209\n"
    "v_cvt_pk_bf16_f32 v209, v210, v211\n"
    "v_pk_mul_f32 v[212:213], v[212:213], v[70:71]\n"
    "v_pk_mul_f32 v[214:215], v[214:215], v[70:71]\n"
    "v_cvt_pk_bf16_f32 v212, v212, v213\n"
    "v_cvt_pk_bf16_f32 v213, v214, v215\n"
    "s_mov_b32 s53, 163840\n"
    "s_waitcnt lgkmcnt(5)\n"
    "buffer_store_dwordx4 v[8:11], v253, s[48:51], s53 offen nt\n"
    "ds_write_b64 v251, v[208:209] offset:128\n"
    "ds_write_b64 v251, v[212:213] offset:160\n"
    "v_pk_mul_f32 v[216:217], v[216:217], v[70:71]\n"
    "v_pk_mul_f32 v[218:219], v[218:219], v[70:71]\n"
    "v_cvt_pk_bf16_f32 v216, v216, v217\n"
    "v_cvt_pk_bf16_f32 v217, v218, v219\n"
    "v_pk_mul_f32 v[220:221], v[220:221], v[70:71]\n"
    "v_pk_mul_f32 v[222:223], v[222:223], v[70:71]\n"
    "v_cvt_pk_bf16_f32 v220, v220, v221\n"
    "v_cvt_pk_bf16_f32 v221, v222, v223\n"
    "s_mov_b32 s53, 180224\n"
    "s_waitcnt lgkmcnt(6)\n"
    "buffer_store_dwordx4 v[12:15], v253, s[48:51], s53 offen nt\n"
    "ds_write_b64 v251, v[216:217] offset:192\n"
    "ds_write_b64 v251, v[220:221] offset:224\n"
    "ds_read_b128 v[16:19], v252 offset:0\n"
    "ds_read_b128 v[20:23], v252 offset:1088\n"
    "ds_read_b128 v[24:27], v252 offset:2176\n"
    "ds_read_b128 v[28:31], v252 offset:3264\n"
    "s_mov_b32 s53, 196608\n"
    "s_waitcnt lgkmcnt(3)\n"
    "buffer_store_dwordx4 v[16:19], v253, s[48:51], s53 offen nt\n"
    "s_mov_b32 s53, 212992\n"
    "s_waitcnt lgkmcnt(2)\n"
    "buffer_store_dwordx4 v[20:23], v253, s[48:51], s53 offen nt\n"
    "s_mov_b32 s53, 229376\n"
    "s_waitcnt lgkmcnt(1)\n"
    "buffer_store_dwordx4 v[24:27], v253, s[48:51], s53 offen nt\n"
    "s_mov_b32 s53, 245760\n"
    "s_waitcnt lgkmcnt(0)\n"
    "buffer_store_dwordx4 v[28:31], v253, s[48:51], s53 offen nt\n"
    "s_or_b32 %0, s76, s77\n"
    "L4_end_%=:\n"
    "Lk_end4:\n"
    "s_nop 1\n"
    : "=&s"(flag) : "s"(kb), "s"(nrk), "s"(vb), "s"(qp), "s"(nrq), "s"(op), "s"(nh), "s"(nv), "s"(w), "s"(ldsb), "s"(nv0), "a"(k0), "a"(k1)
    : "v0", "v1", "v2", "v3", "v4", "v5", "v6", "v7", "v8", "v9", "v10", "v11", "v12", "v13", "v14", "v15", "v16", "v17", "v18", "v19", "v20", "v21", "v22", "v23", "v24", "v25", "v26", "v27", "v28", "v29", "v30", "v31", "v32", "v33", "v34", "v35", "v36", "v37", "v38", "v39", "v40", "v41", "v42", "v43", "v44", "v45", "v46", "v47", "v48", "v49", "v50", "v51", "v52", "v53", "v54", "v55", "v56", "v57", "v58", "v59", "v60", "v61", "v62", "v63", "v64", "v65", "v66", "v67", "v68", "v69", "v70", "v71", "v72", "v73", "v74", "v75", "v76", "v77", "v78", "v79", "v80", "v81", "v82", "v83", "v84", "v85", "v86", "v87", "v88", "v89", "v90", "v91", "v92", "v93", "v94", "v95", "v96", "v97", "v98", "v99", "v100", "v101", "v102", "v103", "v104", "v105", "v106", "v107", "v108", "v109", "v110", "v111", "v112", "v113", "v114", "v115", "v116", "v117", "v118", "v119", "v120", "v121", "v122", "v123", "v124", "v125", "v126", "v127", "v128", "v129", "v130", "v131", "v132", "v133", "v134", "v135", "v136", "v137", "v138", "v139", "v140", "v141", "v142", "v143", "v144", "v145", "v146", "v147", "v148", "v149", "v150", "v151", "v152", "v153", "v154", "v155", "v156", "v157", "v158", "v159", "v160", "v161", "v162", "v163", "v164", "v165", "v166", "v167", "v168", "v169", "v170", "v171", "v172", "v173", "v174", "v175", "v176", "v177", "v178", "v179", "v180", "v181", "v182", "v183", "v184", "v185", "v186", "v187", "v188", "v189", "v190", "v191", "v192", "v193", "v194", "v195", "v196", "v197", "v198", "v199", "v200", "v201", "v202", "v203", "v204", "v205", "v206", "v207", "v208", "v209", "v210", "v211", "v212", "v213", "v214", "v215", "v216", "v217", "v218", "v219", "v220", "v221", "v222", "v223", "v224", "v225", "v226", "v227", "v228", "v229", "v230", "v231", "v232", "v233", "v234", "v235", "v236", "v237", "v238", "v239", "v240", "v241", "v242", "v243", "v244", "v245", "v246", "v247", "v248", "v249", "v250", "v251", "v252", "v253", "a0", "a1", "a2", "a3", "a4", "a5", "a6", "a7", "a8", "a9", "a10", "a11", "a12", "a13", "a14", "a15", "a16", "a17", "a18", "a19", "a20", "a21", "a22", "a23", "a24", "a25", "a26", "a27", "a28", "a29", "a30", "a31", "a32", "a33", "a34", "a35", "a36", "a37", "a38", "a39", "a40", "a41", "a42", "a43", "a44", "a45", "a46", "a47", "a48", "a49", "a50", "a51", "a52", "a53", "a54", "a55", "a56", "a57", "a58", "a59", "a60", "a61", "a62", "a63", "a64", "a65", "a66", "a67", "a68", "a69", "a70", "a71", "a72", "a73", "a74", "a75", "a76", "a77", "a78", "a79", "a80", "a81", "a82", "a83", "a84", "a85", "a86", "a87", "a88", "a89", "a90", "a91", "a92", "a93", "a94", "a95", "a96", "a97", "a98", "a99", "a100", "a101", "a102", "a103", "a104", "a105", "a106", "a107", "a108", "a109", "a110", "a111", "a112", "a113", "a114", "a115", "a116", "a117", "a118", "a119", "a120", "a121", "a122", "a123", "a124", "a125", "a126", "a127", "a128", "a129", "a130", "a131", "a132", "a133", "a134", "a135", "a136", "a137", "a138", "a139", "a140", "a141", "a142", "a143", "a144", "a145", "a146", "a147", "a148", "a152", "a153", "a154", "a155", "a156", "a157", "a158", "a159", "a160", "a161", "a162", "a163", "a164", "a165", "a166", "a167", "a168", "a169", "a170", "a171", "a172", "a173", "a174", "a175", "a176", "a177", "a178", "a179", "a180", "a181", "a182", "a183", "a184", "a185", "a186", "a187", "a188", "a189", "a190", "a191", "a192", "a193", "a194", "a195", "s36", "s37", "s38", "s39", "s40", "s41", "s42", "s43", "s44", "s45", "s46", "s47", "s48", "s49", "s50", "s51", "s52", "s53", "s54", "s55", "s56", "s57", "s58", "s59", "s60", "s61", "s62", "s63", "s64", "s65", "s66", "s67", "s68", "s69", "s70", "s71", "s72", "s73", "s74", "s75", "s76", "s77", "vcc", "scc", "memory");
  return flag;
}

__device__ __forceinline__ unsigned seg3(uint64_t kb, unsigned nrk, uint64_t vb, uint64_t qp, unsigned nrq, uint64_t op, unsigned nh, unsigned nv, unsigned w, unsigned ldsb, unsigned nv0, unsigned k0, unsigned k1) {
  unsigned flag;
  asm volatile(
    "s_mov_b64 s[36:37], %1\n"
    "s_mov_b32 s38, %2\n"
    "s_mov_b32 s39, 0x20000\n"
    "s_mov_b64 s[44:45], %4\n"
    "s_mov_b32 s46, %5\n"
    "s_mov_b32 s47, 0x20000\n"
    "s_mov_b32 s60, %9\n"
    "s_mov_b32 s67, %10\n"
    "v_mbcnt_lo_u32_b32 v236, -1, 0\n"
    "v_mbcnt_hi_u32_b32 v236, -1, v236\n"
    "v_lshrrev_b32 v237, 4, v236\n"
    "v_bfe_u32 v238, v236, 2, 3\n"
    "v_lshlrev_b32 v238, 12, v238\n"
    "v_lshrrev_b32 v239, 5, v236\n"
    "v_lshl_add_u32 v238, v239, 6, v238\n"
    "v_and_b32 v240, 3, v236\n"
    "v_lshl_add_u32 v241, v240, 4, v238\n"
    "s_and_b32 s61, s60, 1\n"
    "s_mul_i32 s61, s61, 48\n"
    "v_xor_b32 v243, s61, v241\n"
    "s_lshl_b32 s61, s60, 15\n"
    "s_add_u32 s61, s61, 4096\n"
    "v_add_u32 v243, s61, v243\n"
    "v_add_u32 v240, -896, v243\n"
    "s_lshl_b32 s61, s60, 11\n"
    "s_add_u32 s61, s61, s67\n"
    "s_add_u32 m0, s61, 73728\n"
    "s_nop 0\n"
    "buffer_load_dwordx4 v243, s[36:39], 0 offen nt lds\n"
    "buffer_load_dwordx4 v240, s[36:39], 0 offen offset:1024 nt lds\n"
    "v_xor_b32 v242, 48, v241\n"
    "s_lshl_b32 s61, s60, 16\n"
    "s_add_u32 s61, s61, 4096\n"
    "v_add_u32 v228, s61, v241\n"
    "v_add_u32 v229, -896, v228\n"
    "v_add_u32 v230, s61, v242\n"
    "v_add_u32 v231, 29824, v230\n"
    "v_add_u32 v230, 30720, v230\n"
    "s_lshl_b32 s54, s60, 12\n"
    "s_add_u32 s54, s54, s67\n"
    "s_mov_b32 s52, 0x20000\n"
    "s_sub_u32 s44, s44, 4096\n"
    "s_subb_u32 s45, s45, 0\n"
    "s_add_u32 s46, s46, 4096\n"
    "s_lshl_b32 s64, s60, 14\n"
    "s_add_u32 s64, s64, 98304\n"
    "s_add_u32 s76, s64, s67\n"
    "s_mul_i32 s77, s60, 131072\n"
    "s_add_u32 m0, s76, 0\n"
    "s_nop 0\n"
    "buffer_load_dwordx4 v228, s[44:47], s77 offen offset:0 nt lds\n"
    "buffer_load_dwordx4 v230, s[44:47], s77 offen offset:2048 nt lds\n"
    "buffer_load_dwordx4 v229, s[44:47], s77 offen offset:1024 nt lds\n"
    "buffer_load_dwordx4 v231, s[44:47], s77 offen offset:3072 nt lds\n"
    "v_accvgpr_write_b32 a96, 0\n"
    "v_accvgpr_write_b32 a97, 0\n"
    "v_accvgpr_write_b32 a98, 0\n"
    "v_accvgpr_write_b32 a99, 0\n"
    "s_add_u32 m0, s54, 0\n"
    "s_nop 0\n"
    "buffer_load_dwordx4 v228, s[36:39], s52 offen offset:0 nt lds\n"
    "s_mov_b64 s[40:41], %3\n"
    "s_mov_b32 s42, %2\n"
    "v_mfma_f32_32x32x16_bf16 v[96:111], a[96:99], a[96:99], 0\n"
    "s_mov_b32 s43, 0x20000\n"
    "v_bfe_u32 v238, v236, 1, 3\n"
    "v_lshlrev_b32 v238, 12, v238\n"
    "v_lshl_add_u32 v238, v237, 5, v238\n"
    "v_and_b32 v239, 1, v236\n"
    "v_lshl_add_u32 v238, v239, 4, v238\n"
    "v_add_u32 v232, s61, v238\n"
    "v_add_u32 v233, -896, v232\n"
    "buffer_load_dwordx4 v229, s[36:39], s52 offen offset:1024 nt lds\n"
    "v_add_u32 v234, 30720, v232\n"
    "v_add_u32 v235, 29824, v232\n"
    "s_add_u32 s55, s54, 16384\n"
    "v_mfma_f32_32x32x16_bf16 v[112:127], a[96:99], a[96:99], 0\n"
    "s_mov_b32 s53, 0\n"
    "s_mov_b64 s[48:49], %6\n"
    "s_mov_b32 s50, %5\n"
    "s_mov_b32 s51, 0x20000\n"
    "s_mov_b32 s66, %7\n"
    "s_mov_b32 s65, %8\n"
    "s_mov_b32 s62, 0x3e0293ee\n"
    "buffer_load_dwordx4 v230, s[36:39], s52 offen offset:2048 nt lds\n"
    "s_mov_b32 s63, 0x3e0293ee\n"
    "v_bfe_u32 v238, v236, 3, 1\n"
    "v_mul_u32_u24 v239, 3, v238\n"
    "v_xor_b32 v239, v239, v237\n"
    "v_mfma_f32_32x32x16_bf16 v[128:143], a[96:99], a[96:99], 0\n"
    "v_lshlrev_b32 v238, 11, v238\n"
    "v_lshl_add_u32 v238, v239, 4, v238\n"
    "v_and_b32 v239, 7, v236\n"
    "v_lshl_add_u32 v238, v239, 6, v238\n"
    "v_add_u32 v226, s67, v238\n"
    "v_lshrrev_b32 v238, 5, v236\n"
    "buffer_load_dwordx4 v231, s[36:39], s52 offen offset:3072 nt lds\n"
    "v_lshlrev_b32 v238, 11, v238\n"
    "v_and_b32 v239, 31, v236\n"
    "v_lshl_add_u32 v238, v239, 3, v238\n"
    "v_add_u32 v238, 16384, v238\n"
    "v_add_u32 v227, s67, v238\n"
    "v_mfma_f32_32x32x16_bf16 v[144:159], a[96:99], a[96:99], 0\n"
    "v_mov_b32 v224, v226\n"
    "v_mov_b32 v225, v227\n"
    "s_mov_b32 s58, 0\n"
    "s_mov_b32 s59, 65536\n"
    "v_lshlrev_b32 v238, 2, v237\n"
    "s_add_u32 m0, s76, 4096\n"
    "s_add_u32 s77, s77, 65536\n"
    "buffer_load_dwordx4 v228, s[44:47], s77 offen offset:0 nt lds\n"
    "v_sub_u32 v248, s65, v238\n"
    "v_sub_u32 v250, %11, v238\n"
    "v_mov_b32 v249, 0xff800000\n"
    "v_and_b32 v238, 3, v236\n"
    "v_cmp_eq_u32 vcc, 0, v238\n"
    "v_mov_b32 v239, 0x3f803f80\n"
    "v_mfma_f32_32x32x16_bf16 v[160:175], a[96:99], a[96:99], 0\n"
    "s_nop 0\n"
    "v_cndmask_b32 v238, 0, v239, vcc\n"
    "v_accvgpr_write_b32 a144, v238\n"
    "v_accvgpr_write_b32 a145, v238\n"
    "buffer_load_dwordx4 v230, s[44:47], s77 offen offset:2048 nt lds\n"
    "v_accvgpr_write_b32 a146, v238\n"
    "v_accvgpr_write_b32 a147, v238\n"
    "v_bfe_u32 v238, v236, 1, 1\n"
    "v_lshrrev_b32 v240, 5, v236\n"
    "v_cmp_eq_u32 vcc, v238, v240\n"
    "v_accvgpr_write_b32 a152, 0\n"
    "v_accvgpr_write_b32 a153, 0\n"
    "v_mfma_f32_32x32x16_bf16 v[176:191], a[96:99], a[96:99], 0\n"
    "v_accvgpr_write_b32 a154, 0\n"
    "v_accvgpr_write_b32 a155, 0\n"
    "v_cndmask_b32 v238, 0, v239, vcc\n"
    "buffer_load_dwordx4 v229, s[44:47], s77 offen offset:1024 nt lds\n"
    "v_accvgpr_write_b32 a192, v238\n"
    "v_accvgpr_write_b32 a193, v238\n"
    "v_accvgpr_write_b32 a194, v238\n"
    "v_accvgpr_write_b32 a195, v238\n"
    "s_cmp_lt_u32 s65, 32\n"
    "s_cselect_b32 s57, 1, 0\n"
    "s_cmp_ge_u32 s66, 3\n"
    "s_cselect_b32 s57, s57, 0\n"
    "v_mfma_f32_32x32x16_bf16 a[128:143], a[96:99], a[96:99], 0\n"
    "s_sub_u32 s56, s66, s57\n"
    "s_sub_u32 s56, s56, 2\n"
    "buffer_load_dwordx4 v231, s[44:47], s77 offen offset:3072 nt lds\n"
    "s_add_u32 s61, s64, s67\n"
    "v_and_b32 v238, 15, v236\n"
    "v_mul_u32_u24 v239, 272, v238\n"
    "v_lshl_add_u32 v239, v237, 3, v239\n"
    "v_add_u32 v251, s61, v239\n"
    "v_mul_u32_u24 v239, 272, v237\n"
    "v_lshl_add_u32 v239, v238, 4, v239\n"
    "v_add_u32 v252, s61, v239\n"
    "s_mul_i32 s61, s60, 48\n"
    "v_add_u32 v239, s61, v237\n"
    "v_lshlrev_b32 v239, 12, v239\n"
    "v_lshl_add_u32 v253, v238, 4, v239\n"
    "v_add_u32 v243, s64, v226\n"
    "v_add_u32 v242, 73728, v226\n"
    "s_waitcnt vmcnt(10)\n"
    "ds_read_b128 v[192:195], v243 offset:0\n"
    "ds_read_b128 v[196:199], v243 offset:512\n"
    "s_waitcnt vmcnt(8)\n"
    "ds_read_b128 v[200:203], v243 offset:1024\n"
    "ds_read_b128 v[204:207], v243 offset:1536\n"
    "s_waitcnt lgkmcnt(2)\n"
    "v_lshlrev_b32 v88, 16, v192\n"
    "v_and_b32 v89, 0xffff0000, v192\n"
    "v_pk_mul_f32 v[88:89], v[88:89], s[62:63]\n"
    "v_cvt_pk_bf16_f32 v192, v88, v89\n"
    "v_lshlrev_b32 v90, 16, v193\n"
    "v_and_b32 v91, 0xffff0000, v193\n"
    "v_pk_mul_f32 v[90:91], v[90:91], s[62:63]\n"
    "v_cvt_pk_bf16_f32 v193, v90, v91\n"
    "s_add_u32 m0, s55, 0\n"
    "s_add_u32 s52, s52, 0x40000\n"
    "buffer_load_dwordx4 v232, s[40:43], s53 offen offset:0 nt lds\n"
    "v_lshlrev_b32 v88, 16, v194\n"
    "v_and_b32 v89, 0xffff0000, v194\n"
    "v_pk_mul_f32 v[88:89], v[88:89], s[62:63]\n"
    "v_cvt_pk_bf16_f32 v194, v88, v89\n"
    "v_lshlrev_b32 v90, 16, v195\n"
    "v_and_b32 v91, 0xffff0000, v195\n"
    "v_pk_mul_f32 v[90:91], v[90:91], s[62:63]\n"
    "v_cvt_pk_bf16_f32 v195, v90, v91\n"
    "buffer_load_dwordx4 v233, s[40:43], s53 offen offset:1024 nt lds\n"
    "v_lshlrev_b32 v88, 16, v196\n"
    "v_and_b32 v89, 0xffff0000, v196\n"
    "v_pk_mul_f32 v[88:89], v[88:89], s[62:63]\n"
    "v_cvt_pk_bf16_f32 v196, v88, v89\n"
    "v_lshlrev_b32 v90, 16, v197\n"
    "v_and_b32 v91, 0xffff0000, v197\n"
    "v_pk_mul_f32 v[90:91], v[90:91], s[62:63]\n"
    "v_cvt_pk_bf16_f32 v197, v90, v91\n"
    "buffer_load_dwordx4 v234, s[40:43], s53 offen offset:2048 nt lds\n"
    "v_lshlrev_b32 v88, 16, v198\n"
    "v_and_b32 v89, 0xffff0000, v198\n"
    "v_pk_mul_f32 v[88:89], v[88:89], s[62:63]\n"
    "v_cvt_pk_bf16_f32 v198, v88, v89\n"
    "v_lshlrev_b32 v90, 16, v199\n"
    "v_and_b32 v91, 0xffff0000, v199\n"
    "v_pk_mul_f32 v[90:91], v[90:91], s[62:63]\n"
    "v_cvt_pk_bf16_f32 v199, v90, v91\n"
    "buffer_load_dwordx4 v235, s[40:43], s53 offen offset:3072 nt lds\n"
    "s_add_u32 s53, s53, 0x40000\n"
    "s_waitcnt vmcnt(8)\n"
    "s_barrier\n"
    "ds_read_b128 a[64:67], v242 offset:0\n"
    "ds_read_b128 a[68:71], v242 offset:512\n"
    "ds_read_b128 a[72:75], v242 offset:1024\n"
    "ds_read_b128 a[76:79], v242 offset:1536\n"
    "ds_read_b128 a[80:83], v242 offset:4096\n"
    "ds_read_b128 a[84:87], v242 offset:4608\n"
    "ds_read_b128 a[88:91], v242 offset:5120\n"
    "ds_read_b128 a[92:95], v242 offset:5632\n"
    "ds_read_b128 a[160:163], v226 offset:0\n"
    "ds_read_b128 a[164:167], v226 offset:512\n"
    "ds_read_b128 a[168:171], v226 offset:1024\n"
    "ds_read_b128 a[172:175], v226 offset:1536\n"
    "ds_read_b128 a[176:179], v226 offset:4096\n"
    "ds_read_b128 a[180:183], v226 offset:4608\n"
    "ds_read_b128 a[184:187], v226 offset:5120\n"
    "ds_read_b128 a[188:191], v226 offset:5632\n"
    "v_and_b32 v242, 1, v236\n"
    "v_mul_u32_u24 v242, 0xaaaa, v242\n"
    "v_add_u32 v242, 0x4444, v242\n"
    "v_lshl_or_b32 v242, v242, 16, v242\n"
    "s_waitcnt lgkmcnt(15)\n"
    "v_lshlrev_b32 v88, 16, v200\n"
    "v_and_b32 v89, 0xffff0000, v200\n"
    "v_pk_mul_f32 v[88:89], v[88:89], s[62:63]\n"
    "v_cvt_pk_bf16_f32 v200, v88, v89\n"
    "v_lshlrev_b32 v90, 16, v201\n"
    "v_and_b32 v91, 0xffff0000, v201\n"
    "v_pk_mul_f32 v[90:91], v[90:91], s[62:63]\n"
    "v_cvt_pk_bf16_f32 v201, v90, v91\n"
    "s_add_u32 m0, s76, 8192\n"
    "s_add_u32 s77, s77, 65536\n"
    "buffer_load_dwordx4 v228, s[44:47], s77 offen offset:0 nt lds\n"
    "v_lshlrev_b32 v88, 16, v202\n"
    "v_and_b32 v89, 0xffff0000, v202\n"
    "v_pk_mul_f32 v[88:89], v[88:89], s[62:63]\n"
    "v_cvt_pk_bf16_f32 v202, v88, v89\n"
    "v_lshlrev_b32 v90, 16, v203\n"
    "v_and_b32 v91, 0xffff0000, v203\n"
    "v_pk_mul_f32 v[90:91], v[90:91], s[62:63]\n"
    "v_cvt_pk_bf16_f32 v203, v90, v91\n"
    "buffer_load_dwordx4 v230, s[44:47], s77 offen offset:2048 nt lds\n"
    "v_lshlrev_b32 v88, 16, v204\n"
    "v_and_b32 v89, 0xffff0000, v204\n"
    "v_pk_mul_f32 v[88:89], v[88:89], s[62:63]\n"
    "v_cvt_pk_bf16_f32 v204, v88, v89\n"
    "v_lshlrev_b32 v90, 16, v205\n"
    "v_and_b32 v91, 0xffff0000, v205\n"
    "v_pk_mul_f32 v[90:91], v[90:91], s[62:63]\n"
    "v_cvt_pk_bf16_f32 v205, v90, v91\n"
    "buffer_load_dwordx4 v229, s[44:47], s77 offen offset:1024 nt lds\n"
    "v_lshlrev_b32 v88, 16, v206\n"
    "v_and_b32 v89, 0xffff0000, v206\n"
    "v_pk_mul_f32 v[88:89], v[88:89], s[62:63]\n"
    "v_cvt_pk_bf16_f32 v206, v88, v89\n"
    "v_lshlrev_b32 v90, 16, v207\n"
    "v_and_b32 v91, 0xffff0000, v207\n"
    "v_pk_mul_f32 v[90:91], v[90:91], s[62:63]\n"
    "v_cvt_pk_bf16_f32 v207, v90, v91\n"
    "buffer_load_dwordx4 v231, s[44:47], s77 offen offset:3072 nt lds\n"
    "s_waitcnt lgkmcnt(8)\n"
    "v_mfma_f32_16x16x32_bf16 v[0:3], a[64:67], v[192:195], 0\n"
    "v_mfma_f32_16x16x32_bf16 v[0:3], a[68:71], v[196:199], v[0:3]\n"
    "v_mfma_f32_16x16x32_bf16 v[0:3], a[72:75], v[200:203], v[0:3]\n"
    "v_mfma_f32_16x16x32_bf16 v[0:3], a[76:79], v[204:207], v[0:3]\n"
    "v_mfma_f32_16x16x32_bf16 v[4:7], a[80:83], v[192:195], 0\n"
    "v_mfma_f32_16x16x32_bf16 v[4:7], a[84:87], v[196:199], v[4:7]\n"
    "v_mfma_f32_16x16x32_bf16 v[4:7], a[88:91], v[200:203], v[4:7]\n"
    "v_mfma_f32_16x16x32_bf16 v[4:7], a[92:95], v[204:207], v[4:7]\n"
    "s_cmp_lt_u32 %11, 32\n"
    "s_cbranch_scc1 L3_m0_0x_%=\n"
    "L3_m0_0_%=:\n"
    "s_waitcnt lgkmcnt(0)\n"
    "s_nop 4\n"
    "v_max3_f32 v244, v0, v1, v2\n"
    "v_max3_f32 v238, v3, v4, v5\n"
    "v_max3_f32 v244, v244, v6, v7\n"
    "v_max_f32 v244, v244, v238\n"
    "v_mov_b32 v238, v244\n"
    "s_nop 1\n"
    "v_permlane16_swap_b32 v244, v238\n"
    "s_nop 1\n"
    "v_max_f32 v244, v244, v238\n"
    "v_mov_b32 v238, v244\n"
    "s_nop 1\n"
    "v_permlane32_swap_b32 v244, v238\n"
    "s_nop 1\n"
    "v_max_f32 v244, v244, v238\n"
    "v_sub_f32 v64, 0, v244\n"
    "v_sub_f32 v65, 0, v244\n"
    "v_sub_f32 v66, 0, v244\n"
    "v_sub_f32 v67, 0, v244\n"
    "v_sub_f32 v68, 0, v244\n"
    "v_sub_f32 v69, 0, v244\n"
    "v_sub_f32 v70, 0, v244\n"
    "v_sub_f32 v71, 0, v244\n"
    "s_cmp_eq_u32 s66, 2\n"
    "s_cbranch_scc1 L3_m1_0x_%=\n"
    "L3_m1_0_%=:\n"
    "s_nop 1\n"
    "v_mfma_f32_16x16x32_bf16 v[32:35], a[160:163], v[192:195], v[64:67]\n"
    "v_sub_f32 v0, v0, v244\n"
    "v_sub_f32 v1, v1, v244\n"
    "v_mfma_f32_16x16x32_bf16 v[32:35], a[164:167], v[196:199], v[32:35]\n"
    "v_sub_f32 v2, v2, v244\n"
    "v_sub_f32 v3, v3, v244\n"
    "v_mfma_f32_16x16x32_bf16 v[32:35], a[168:171], v[200:203], v[32:35]\n"
    "v_exp_f32 v0, v0\n"
    "v_mfma_f32_16x16x32_bf16 v[32:35], a[172:175], v[204:207], v[32:35]\n"
    "v_exp_f32 v1, v1\n"
    "v_mfma_f32_16x16x32_bf16 v[36:39], a[176:179], v[192:195], v[68:71]\n"
    "v_exp_f32 v2, v2\n"
    "v_mfma_f32_16x16x32_bf16 v[36:39], a[180:183], v[196:199], v[36:39]\n"
    "v_exp_f32 v3, v3\n"
    "v_mfma_f32_16x16x32_bf16 v[36:39], a[184:187], v[200:203], v[36:39]\n"
    "v_cvt_pk_bf16_f32 v0, v0, v1\n"
    "v_cvt_pk_bf16_f32 v1, v2, v3\n"
    "v_mfma_f32_16x16x32_bf16 v[36:39], a[188:191], v[204:207], v[36:39]\n"
    "v_sub_f32 v4, v4, v244\n"
    "v_sub_f32 v5, v5, v244\n"
    "s_waitcnt vmcnt(10)\n"
    "ds_read_b128 v[208:211], v243 offset:4096\n"
    "ds_read_b128 v[212:215], v243 offset:4608\n"
    "s_waitcnt vmcnt(8)\n"
    "ds_read_b128 v[216:219], v243 offset:5120\n"
    "ds_read_b128 v[220:223], v243 offset:5632\n"
    "s_waitcnt lgkmcnt(2)\n"
    "v_lshlrev_b32 v88, 16, v208\n"
    "v_and_b32 v89, 0xffff0000, v208\n"
    "v_pk_mul_f32 v[88:89], v[88:89], s[62:63]\n"
    "v_cvt_pk_bf16_f32 v208, v88, v89\n"
    "v_lshlrev_b32 v90, 16, v209\n"
    "v_and_b32 v91, 0xffff0000, v209\n"
    "v_pk_mul_f32 v[90:91], v[90:91], s[62:63]\n"
    "v_cvt_pk_bf16_f32 v209, v90, v91\n"
    "s_add_u32 m0, s54, 32768\n"
    "s_nop 0\n"
    "buffer_load_dwordx4 v228, s[36:39], s52 offen offset:0 nt lds\n"
    "v_lshlrev_b32 v88, 16, v210\n"
    "v_and_b32 v89, 0xffff0000, v210\n"
    "v_pk_mul_f32 v[88:89], v[88:89], s[62:63]\n"
    "v_cvt_pk_bf16_f32 v210, v88, v89\n"
    "v_lshlrev_b32 v90, 16, v211\n"
    "v_and_b32 v91, 0xffff0000, v211\n"
    "v_pk_mul_f32 v[90:91], v[90:91], s[62:63]\n"
    "v_cvt_pk_bf16_f32 v211, v90, v91\n"
    "buffer_load_dwordx4 v229, s[36:39], s52 offen offset:1024 nt lds\n"
    "v_lshlrev_b32 v88, 16, v212\n"
    "v_and_b32 v89, 0xffff0000, v212\n"
    "v_pk_mul_f32 v[88:89], v[88:89], s[62:63]\n"
    "v_cvt_pk_bf16_f32 v212, v88, v89\n"
    "v_lshlrev_b32 v90, 16, v213\n"
    "v_and_b32 v91, 0xffff0000, v213\n"
    "v_pk_mul_f32 v[90:91], v[90:91], s[62:63]\n"
    "v_cvt_pk_bf16_f32 v213, v90, v91\n"
    "buffer_load_dwordx4 v230, s[36:39], s52 offen offset:2048 nt lds\n"
    "v_lshlrev_b32 v88, 16, v214\n"
    "v_and_b32 v89, 0xffff0000, v214\n"
    "v_pk_mul_f32 v[88:89], v[88:89], s[62:63]\n"
    "v_cvt_pk_bf16_f32 v214, v88, v89\n"
    "v_lshlrev_b32 v90, 16, v215\n"
    "v_and_b32 v91, 0xffff0000, v215\n"
    "v_pk_mul_f32 v[90:91], v[90:91], s[62:63]\n"
    "v_cvt_pk_bf16_f32 v215, v90, v91\n"
    "buffer_load_dwordx4 v231, s[36:39], s52 offen offset:3072 nt lds\n"
    "s_waitcnt lgkmcnt(0)\n"
    "v_lshlrev_b32 v88, 16, v216\n"
    "v_and_b32 v89, 0xffff0000, v216\n"
    "v_pk_mul_f32 v[88:89], v[88:89], s[62:63]\n"
    "v_cvt_pk_bf16_f32 v216, v88, v89\n"
    "v_lshlrev_b32 v90, 16, v217\n"
    "v_and_b32 v91, 0xffff0000, v217\n"
    "v_pk_mul_f32 v[90:91], v[90:91], s[62:63]\n"
    "v_cvt_pk_bf16_f32 v217, v90, v91\n"
    "s_add_u32 m0, s55, 32768\n"
    "s_add_u32 s52, s52, 0x40000\n"
    "buffer_load_dwordx4 v232, s[40:43], s53 offen offset:0 nt lds\n"
    "v_lshlrev_b32 v88, 16, v218\n"
    "v_and_b32 v89, 0xffff0000, v218\n"
    "v_pk_mul_f32 v[88:89], v[88:89], s[62:63]\n"
    "v_cvt_pk_bf16_f32 v218, v88, v89\n"
    "v_lshlrev_b32 v90, 16, v219\n"
    "v_and_b32 v91, 0xffff0000, v219\n"
    "v_pk_mul_f32 v[90:91], v[90:91], s[62:63]\n"
    "v_cvt_pk_bf16_f32 v219, v90, v91\n"
    "buffer_load_dwordx4 v233, s[40:43], s53 offen offset:1024 nt lds\n"
    "v_lshlrev_b32 v88, 16, v220\n"
    "v_and_b32 v89, 0xffff0000, v220\n"
    "v_pk_mul_f32 v[88:89], v[88:89], s[62:63]\n"
    "v_cvt_pk_bf16_f32 v220, v88, v89\n"
    "v_lshlrev_b32 v90, 16, v221\n"
    "v_and_b32 v91, 0xffff0000, v221\n"
    "v_pk_mul_f32 v[90:91], v[90:91], s[62:63]\n"
    "v_cvt_pk_bf16_f32 v221, v90, v91\n"
    "buffer_load_dwordx4 v234, s[40:43], s53 offen offset:2048 nt lds\n"
    "v_lshlrev_b32 v88, 16, v222\n"
    "v_and_b32 v89, 0xffff0000, v222\n"
    "v_pk_mul_f32 v[88:89], v[88:89], s[62:63]\n"
    "v_cvt_pk_bf16_f32 v222, v88, v89\n"
    "v_lshlrev_b32 v90, 16, v223\n"
    "v_and_b32 v91, 0xffff0000, v223\n"
    "v_pk_mul_f32 v[90:91], v[90:91], s[62:63]\n"
    "v_cvt_pk_bf16_f32 v223, v90, v91\n"
    "buffer_load_dwordx4 v235, s[40:43], s53 offen offset:3072 nt lds\n"
    "s_add_u32 s53, s53, 0x40000\n"
    "s_nop 1\n"
    "v_mfma_f32_16x16x32_bf16 v[8:11], a[64:67], v[208:211], 0\n"
    "v_sub_f32 v6, v6, v244\n"
    "v_sub_f32 v7, v7, v244\n"
    "v_mfma_f32_16x16x32_bf16 v[8:11], a[68:71], v[212:215], v[8:11]\n"
    "v_exp_f32 v4, v4\n"
    "v_mfma_f32_16x16x32_bf16 v[8:11], a[72:75], v[216:219], v[8:11]\n"
    "v_exp_f32 v5, v5\n"
    "v_mfma_f32_16x16x32_bf16 v[8:11], a[76:79], v[220:223], v[8:11]\n"
    "v_exp_f32 v6, v6\n"
    "v_mfma_f32_16x16x32_bf16 v[12:15], a[80:83], v[208:211], 0\n"
    "v_exp_f32 v7, v7\n"
    "v_mfma_f32_16x16x32_bf16 v[12:15], a[84:87], v[212:215], v[12:15]\n"
    "v_cvt_pk_bf16_f32 v2, v4, v5\n"
    "v_cvt_pk_bf16_f32 v3, v6, v7\n"
    "v_mfma_f32_16x16x32_bf16 v[12:15], a[88:91], v[216:219], v[12:15]\n"
    "v_mfma_f32_16x16x32_bf16 v[12:15], a[92:95], v[220:223], v[12:15]\n"
    "s_cmp_lt_u32 %11, 32\n"
    "s_cbranch_scc1 L3_m0_1x_%=\n"
    "L3_m0_1_%=:\n"
    "s_nop 5\n"
    "v_max3_f32 v245, v8, v9, v10\n"
    "v_max3_f32 v239, v11, v12, v13\n"
    "v_max3_f32 v245, v245, v14, v15\n"
    "v_max_f32 v245, v245, v239\n"
    "v_mov_b32 v239, v245\n"
    "s_nop 1\n"
    "v_permlane16_swap_b32 v245, v239\n"
    "s_nop 1\n"
    "v_max_f32 v245, v245, v239\n"
    "v_mov_b32 v239, v245\n"
    "s_nop 1\n"
    "v_permlane32_swap_b32 v245, v239\n"
    "s_nop 1\n"
    "v_max_f32 v245, v245, v239\n"
    "v_sub_f32 v72, 0, v245\n"
    "v_sub_f32 v73, 0, v245\n"
    "v_sub_f32 v74, 0, v245\n"
    "v_sub_f32 v75, 0, v245\n"
    "v_sub_f32 v76, 0, v245\n"
    "v_sub_f32 v77, 0, v245\n"
    "v_sub_f32 v78, 0, v245\n"
    "v_sub_f32 v79, 0, v245\n"
    "s_cmp_eq_u32 s66, 2\n"
    "s_cbranch_scc1 L3_m1_1x_%=\n"
    "L3_m1_1_%=:\n"
    "s_nop 1\n"
    "v_mfma_f32_16x16x32_bf16 v[40:43], a[160:163], v[208:211], v[72:75]\n"
    "v_sub_f32 v14, v14, v245\n"
    "v_sub_f32 v15, v15, v245\n"
    "v_mfma_f32_16x16x32_bf16 v[40:43], a[164:167], v[212:215], v[40:43]\n"
    "v_sub_f32 v12, v12, v245\n"
    "v_sub_f32 v13, v13, v245\n"
    "v_mfma_f32_16x16x32_bf16 v[40:43], a[168:171], v[216:219], v[40:43]\n"
    "v_exp_f32 v14, v14\n"
    "v_mfma_f32_16x16x32_bf16 v[40:43], a[172:175], v[220:223], v[40:43]\n"
    "v_exp_f32 v15, v15\n"
    "v_mfma_f32_16x16x32_bf16 v[44:47], a[176:179], v[208:211], v[76:79]\n"
    "v_exp_f32 v12, v12\n"
    "v_mfma_f32_16x16x32_bf16 v[44:47], a[180:183], v[212:215], v[44:47]\n"
    "v_exp_f32 v13, v13\n"
    "v_mfma_f32_16x16x32_bf16 v[44:47], a[184:187], v[216:219], v[44:47]\n"
    "v_cvt_pk_bf16_f32 v15, v14, v15\n"
    "v_cvt_pk_bf16_f32 v14, v12, v13\n"
    "v_mfma_f32_16x16x32_bf16 v[44:47], a[188:191], v[220:223], v[44:47]\n"
    "v_sub_f32 v10, v10, v245\n"
    "v_sub_f32 v11, v11, v245\n"
    "s_waitcnt vmcnt(12)\n"
    "s_barrier\n"
    "ds_read_b64_tr_b16 a[96:97], v225 offset:0\n"
    "ds_read_b64_tr_b16 a[98:99], v225 offset:4096\n"
    "ds_read_b64_tr_b16 a[100:101], v225 offset:256\n"
    "ds_read_b64_tr_b16 a[102:103], v225 offset:4352\n"
    "ds_read_b64_tr_b16 a[104:105], v225 offset:512\n"
    "ds_read_b64_tr_b16 a[106:107], v225 offset:4608\n"
    "ds_read_b64_tr_b16 a[108:109], v225 offset:768\n"
    "ds_read_b64_tr_b16 a[110:111], v225 offset:4864\n"
    "ds_read_b64_tr_b16 a[112:113], v225 offset:1024\n"
    "ds_read_b64_tr_b16 a[114:115], v225 offset:5120\n"
    "ds_read_b64_tr_b16 a[116:117], v225 offset:1280\n"
    "ds_read_b64_tr_b16 a[118:119], v225 offset:5376\n"
    "ds_read_b64_tr_b16 a[120:121], v225 offset:1536\n"
    "ds_read_b64_tr_b16 a[122:123], v225 offset:5632\n"
    "ds_read_b64_tr_b16 a[124:125], v225 offset:1792\n"
    "ds_read_b64_tr_b16 a[126:127], v225 offset:5888\n"
    "s_waitcnt vmcnt(10)\n"
    "ds_read_b128 v[24:27], v243 offset:8192\n"
    "ds_read_b128 v[28:31], v243 offset:8704\n"
    "s_waitcnt vmcnt(8)\n"
    "ds_read_b128 v[56:59], v243 offset:9216\n"
    "ds_read_b128 v[60:63], v243 offset:9728\n"
    "s_waitcnt lgkmcnt(2)\n"
    "v_lshlrev_b32 v88, 16, v24\n"
    "v_and_b32 v89, 0xffff0000, v24\n"
    "v_pk_mul_f32 v[88:89], v[88:89], s[62:63]\n"
    "v_cvt_pk_bf16_f32 v24, v88, v89\n"
    "v_lshlrev_b32 v90, 16, v25\n"
    "v_and_b32 v91, 0xffff0000, v25\n"
    "v_pk_mul_f32 v[90:91], v[90:91], s[62:63]\n"
    "v_cvt_pk_bf16_f32 v25, v90, v91\n"
    "v_lshlrev_b32 v88, 16, v26\n"
    "v_and_b32 v89, 0xffff0000, v26\n"
    "v_pk_mul_f32 v[88:89], v[88:89], s[62:63]\n"
    "v_cvt_pk_bf16_f32 v26, v88, v89\n"
    "v_lshlrev_b32 v90, 16, v27\n"
    "v_and_b32 v91, 0xffff0000, v27\n"
    "v_pk_mul_f32 v[90:91], v[90:91], s[62:63]\n"
    "v_cvt_pk_bf16_f32 v27, v90, v91\n"
    "v_lshlrev_b32 v88, 16, v28\n"
    "v_and_b32 v89, 0xffff0000, v28\n"
    "v_pk_mul_f32 v[88:89], v[88:89], s[62:63]\n"
    "v_cvt_pk_bf16_f32 v28, v88, v89\n"
    "v_lshlrev_b32 v90, 16, v29\n"
    "v_and_b32 v91, 0xffff0000, v29\n"
    "v_pk_mul_f32 v[90:91], v[90:91], s[62:63]\n"
    "v_cvt_pk_bf16_f32 v29, v90, v91\n"
    "v_lshlrev_b32 v88, 16, v30\n"
    "v_and_b32 v89, 0xffff0000, v30\n"
    "v_pk_mul_f32 v[88:89], v[88:89], s[62:63]\n"
    "v_cvt_pk_bf16_f32 v30, v88, v89\n"
    "v_lshlrev_b32 v90, 16, v31\n"
    "v_and_b32 v91, 0xffff0000, v31\n"
    "v_pk_mul_f32 v[90:91], v[90:91], s[62:63]\n"
    "v_cvt_pk_bf16_f32 v31, v90, v91\n"
    "s_waitcnt lgkmcnt(0)\n"
    "v_lshlrev_b32 v88, 16, v56\n"
    "v_and_b32 v89, 0xffff0000, v56\n"
    "v_pk_mul_f32 v[88:89], v[88:89], s[62:63]\n"
    "v_cvt_pk_bf16_f32 v56, v88, v89\n"
    "v_lshlrev_b32 v90, 16, v57\n"
    "v_and_b32 v91, 0xffff0000, v57\n"
    "v_pk_mul_f32 v[90:91], v[90:91], s[62:63]\n"
    "v_cvt_pk_bf16_f32 v57, v90, v91\n"
    "v_lshlrev_b32 v88, 16, v58\n"
    "v_and_b32 v89, 0xffff0000, v58\n"
    "v_pk_mul_f32 v[88:89], v[88:89], s[62:63]\n"
    "v_cvt_pk_bf16_f32 v58, v88, v89\n"
    "v_lshlrev_b32 v90, 16, v59\n"
    "v_and_b32 v91, 0xffff0000, v59\n"
    "v_pk_mul_f32 v[90:91], v[90:91], s[62:63]\n"
    "v_cvt_pk_bf16_f32 v59, v90, v91\n"
    "v_lshlrev_b32 v88, 16, v60\n"
    "v_and_b32 v89, 0xffff0000, v60\n"
    "v_pk_mul_f32 v[88:89], v[88:89], s[62:63]\n"
    "v_cvt_pk_bf16_f32 v60, v88, v89\n"
    "v_lshlrev_b32 v90, 16, v61\n"
    "v_and_b32 v91, 0xffff0000, v61\n"
    "v_pk_mul_f32 v[90:91], v[90:91], s[62:63]\n"
    "v_cvt_pk_bf16_f32 v61, v90, v91\n"
    "v_lshlrev_b32 v88, 16, v62\n"
    "v_and_b32 v89, 0xffff0000, v62\n"
    "v_pk_mul_f32 v[88:89], v[88:89], s[62:63]\n"
    "v_cvt_pk_bf16_f32 v62, v88, v89\n"
    "v_lshlrev_b32 v90, 16, v63\n"
    "v_and_b32 v91, 0xffff0000, v63\n"
    "v_pk_mul_f32 v[90:91], v[90:91], s[62:63]\n"
    "v_cvt_pk_bf16_f32 v63, v90, v91\n"
    "s_nop 1\n"
    "v_mfma_f32_16x16x32_bf16 v[16:19], a[64:67], v[24:27], 0\n"
    "v_sub_f32 v8, v8, v245\n"
    "v_sub_f32 v9, v9, v245\n"
    "v_mfma_f32_16x16x32_bf16 v[16:19], a[68:71], v[28:31], v[16:19]\n"
    "v_exp_f32 v10, v10\n"
    "v_mfma_f32_16x16x32_bf16 v[16:19], a[72:75], v[56:59], v[16:19]\n"
    "v_exp_f32 v11, v11\n"
    "v_mfma_f32_16x16x32_bf16 v[16:19], a[76:79], v[60:63], v[16:19]\n"
    "v_exp_f32 v8, v8\n"
    "v_mfma_f32_16x16x32_bf16 v[20:23], a[80:83], v[24:27], 0\n"
    "v_exp_f32 v9, v9\n"
    "v_mfma_f32_16x16x32_bf16 v[20:23], a[84:87], v[28:31], v[20:23]\n"
    "v_cvt_pk_bf16_f32 v13, v10, v11\n"
    "v_cvt_pk_bf16_f32 v12, v8, v9\n"
    "v_mfma_f32_16x16x32_bf16 v[20:23], a[88:91], v[56:59], v[20:23]\n"
    "v_mfma_f32_16x16x32_bf16 v[20:23], a[92:95], v[60:63], v[20:23]\n"
    "s_cmp_lt_u32 %11, 32\n"
    "s_cbranch_scc1 L3_m0_2x_%=\n"
    "L3_m0_2_%=:\n"
    "s_nop 5\n"
    "v_max3_f32 v246, v16, v17, v18\n"
    "v_max3_f32 v240, v19, v20, v21\n"
    "v_max3_f32 v246, v246, v22, v23\n"
    "v_max_f32 v246, v246, v240\n"
    "v_mov_b32 v240, v246\n"
    "s_nop 1\n"
    "v_permlane16_swap_b32 v246, v240\n"
    "s_nop 1\n"
    "v_max_f32 v246, v246, v240\n"
    "v_mov_b32 v240, v246\n"
    "s_nop 1\n"
    "v_permlane32_swap_b32 v246, v240\n"
    "s_nop 1\n"
    "v_max_f32 v246, v246, v240\n"
    "v_sub_f32 v80, 0, v246\n"
    "v_sub_f32 v81, 0, v246\n"
    "v_sub_f32 v82, 0, v246\n"
    "v_sub_f32 v83, 0, v246\n"
    "v_sub_f32 v84, 0, v246\n"
    "v_sub_f32 v85, 0, v246\n"
    "v_sub_f32 v86, 0, v246\n"
    "v_sub_f32 v87, 0, v246\n"
    "s_cmp_eq_u32 s66, 2\n"
    "s_cbranch_scc1 L3_m1_2x_%=\n"
    "L3_m1_2_%=:\n"
    "s_nop 1\n"
    "v_mfma_f32_16x16x32_bf16 v[48:51], a[160:163], v[24:27], v[80:83]\n"
    "v_sub_f32 v16, v16, v246\n"
    "v_sub_f32 v17, v17, v246\n"
    "v_mfma_f32_16x16x32_bf16 v[48:51], a[164:167], v[28:31], v[48:51]\n"
    "v_sub_f32 v18, v18, v246\n"
    "v_sub_f32 v19, v19, v246\n"
    "v_mfma_f32_16x16x32_bf16 v[48:51], a[168:171], v[56:59], v[48:51]\n"
    "v_exp_f32 v16, v16\n"
    "v_mfma_f32_16x16x32_bf16 v[48:51], a[172:175], v[60:63], v[48:51]\n"
    "v_exp_f32 v17, v17\n"
    "v_mfma_f32_16x16x32_bf16 v[52:55], a[176:179], v[24:27], v[84:87]\n"
    "v_exp_f32 v18, v18\n"
    "v_mfma_f32_16x16x32_bf16 v[52:55], a[180:183], v[28:31], v[52:55]\n"
    "v_exp_f32 v19, v19\n"
    "v_mfma_f32_16x16x32_bf16 v[52:55], a[184:187], v[56:59], v[52:55]\n"
    "v_cvt_pk_bf16_f32 v16, v16, v17\n"
    "v_cvt_pk_bf16_f32 v17, v18, v19\n"
    "v_mfma_f32_16x16x32_bf16 v[52:55], a[188:191], v[60:63], v[52:55]\n"
    "v_sub_f32 v20, v20, v246\n"
    "v_sub_f32 v21, v21, v246\n"
    "s_add_u32 m0, s54, s59\n"
    "v_sub_f32 v22, v22, v246\n"
    "v_sub_f32 v23, v23, v246\n"
    "v_exp_f32 v20, v20\n"
    "buffer_load_dwordx4 v228, s[36:39], s52 offen offset:0 nt lds\n"
    "v_exp_f32 v21, v21\n"
    "v_exp_f32 v22, v22\n"
    "v_exp_f32 v23, v23\n"
    "buffer_load_dwordx4 v229, s[36:39], s52 offen offset:1024 nt lds\n"
    "v_cvt_pk_bf16_f32 v18, v20, v21\n"
    "v_cvt_pk_bf16_f32 v19, v22, v23\n"
    "buffer_load_dwordx4 v230, s[36:39], s52 offen offset:2048 nt lds\n"
    "buffer_load_dwordx4 v231, s[36:39], s52 offen offset:3072 nt lds\n"
    "s_add_u32 s52, s52, 0x40000\n"
    "s_waitcnt lgkmcnt(0)\n"
    "s_sub_u32 s56, s56, 1\n"
    "s_cbranch_scc1 L3_ev0_%=\n"
    "s_branch L3_p1_%=\n"
    ".p2align 6\n"
    "L3_p0_%=:\n"
    "v_mfma_f32_16x16x32_bf16 v[96:99], a[96:99], v[32:35], v[96:99]\n"
    "s_waitcnt vmcnt(8)\n"
    "s_barrier\n"
    "v_mfma_f32_16x16x32_bf16 v[100:103], a[100:103], v[32:35], v[100:103]\n"
    "s_add_u32 m0, s54, s59\n"
    "v_exp_f32 v0, v0\n"
    "v_mfma_f32_16x16x32_bf16 v[104:107], a[104:107], v[32:35], v[104:107]\n"
    "v_exp_f32 v1, v1\n"
    "v_mfma_f32_16x16x32_bf16 v[108:111], a[108:111], v[32:35], v[108:111]\n"
    "ds_read_b128 a[64:67], v224 offset:0\n"
    "v_cvt_pk_bf16_f32 v0, v0, v1\n"
    "v_mfma_f32_16x16x32_bf16 v[112:115], a[112:115], v[32:35], v[112:115]\n"
    "v_exp_f32 v2, v2\n"
    "v_mfma_f32_16x16x32_bf16 v[116:119], a[116:119], v[32:35], v[116:119]\n"
    "ds_read_b128 a[68:71], v224 offset:512\n"
    "v_mfma_f32_16x16x32_bf16 v[120:123], a[120:123], v[32:35], v[120:123]\n"
    "v_exp_f32 v3, v3\n"
    "v_mfma_f32_16x16x32_bf16 v[124:127], a[124:127], v[32:35], v[124:127]\n"
    "ds_read_b128 a[72:75], v224 offset:1024\n"
    "v_cvt_pk_bf16_f32 v1, v2, v3\n"
    "v_mfma_f32_16x16x32_bf16 a[128:131], a[144:147], v[32:35], a[128:131]\n"
    "v_exp_f32 v4, v4\n"
    "v_mfma_f32_16x16x32_bf16 v[128:131], a[96:99], v[44:47], v[128:131]\n"
    "ds_read_b128 a[76:79], v224 offset:1536\n"
    "v_mfma_f32_16x16x32_bf16 v[132:135], a[100:103], v[44:47], v[132:135]\n"
    "v_exp_f32 v5, v5\n"
    "v_mfma_f32_16x16x32_bf16 v[136:139], a[104:107], v[44:47], v[136:139]\n"
    "buffer_load_dwordx4 v228, s[36:39], s52 offen offset:0 lds\n"
    "v_mfma_f32_16x16x32_bf16 v[140:143], a[108:111], v[44:47], v[140:143]\n"
    "v_exp_f32 v6, v6\n"
    "v_mfma_f32_16x16x32_bf16 v[144:147], a[112:115], v[44:47], v[144:147]\n"
    "ds_read_b128 a[80:83], v224 offset:4096\n"
    "v_cvt_pk_bf16_f32 v2, v4, v5\n"
    "v_mfma_f32_16x16x32_bf16 v[148:151], a[116:119], v[44:47], v[148:151]\n"
    "v_exp_f32 v7, v7\n"
    "v_mfma_f32_16x16x32_bf16 v[152:155], a[120:123], v[44:47], v[152:155]\n"
    "ds_read_b128 a[84:87], v224 offset:4608\n"
    "v_cvt_pk_bf16_f32 v3, v6, v7\n"
    "v_mfma_f32_16x16x32_bf16 v[156:159], a[124:127], v[44:47], v[156:159]\n"
    "v_exp_f32 v14, v14\n"
    "v_mfma_f32_16x16x32_bf16 v[160:163], a[96:99], v[48:51], v[160:163]\n"
    "buffer_load_dwordx4 v229, s[36:39], s52 offen offset:1024 lds\n"
    "v_mfma_f32_16x16x32_bf16 v[164:167], a[100:103], v[48:51], v[164:167]\n"
    "v_exp_f32 v15, v15\n"
    "v_mfma_f32_16x16x32_bf16 v[168:171], a[104:107], v[48:51], v[168:171]\n"
    "ds_read_b128 a[88:91], v224 offset:5120\n"
    "v_cvt_pk_bf16_f32 v15, v14, v15\n"
    "v_mfma_f32_16x16x32_bf16 v[172:175], a[108:111], v[48:51], v[172:175]\n"
    "v_exp_f32 v12, v12\n"
    "v_mfma_f32_16x16x32_bf16 v[176:179], a[112:115], v[48:51], v[176:179]\n"
    "ds_read_b128 a[92:95], v224 offset:5632\n"
    "v_mfma_f32_16x16x32_bf16 v[180:183], a[116:119], v[48:51], v[180:183]\n"
    "buffer_load_dwordx4 v230, s[36:39], s52 offen offset:2048 lds\n"
    "v_exp_f32 v13, v13\n"
    "v_mfma_f32_16x16x32_bf16 v[184:187], a[120:123], v[48:51], v[184:187]\n"
    "ds_read_b64_tr_b16 a[96:97], v225 offset:0\n"
    "ds_read_b64_tr_b16 a[98:99], v225 offset:4096\n"
    "v_mfma_f32_16x16x32_bf16 v[188:191], a[124:127], v[48:51], v[188:191]\n"
    "buffer_load_dwordx4 v231, s[36:39], s52 offen offset:3072 lds\n"
    "v_smfmac_f32_16x16x64_bf16 a[152:155], a[192:195], v[44:51], v242\n"
    "s_waitcnt lgkmcnt(2)\n"
    "v_exp_f32 v10, v10\n"
    "v_mfma_f32_16x16x32_bf16 v[32:35], a[64:67], v[192:195], v[64:67]\n"
    "ds_read_b64_tr_b16 a[100:101], v225 offset:256\n"
    "v_cvt_pk_bf16_f32 v14, v12, v13\n"
    "v_mfma_f32_16x16x32_bf16 v[32:35], a[68:71], v[196:199], v[32:35]\n"
    "s_add_u32 s52, s52, 0x40000\n"
    "v_exp_f32 v11, v11\n"
    "v_mfma_f32_16x16x32_bf16 v[32:35], a[72:75], v[200:203], v[32:35]\n"
    "ds_read_b64_tr_b16 a[102:103], v225 offset:4352\n"
    "ds_read_b64_tr_b16 a[104:105], v225 offset:512\n"
    "v_mfma_f32_16x16x32_bf16 v[32:35], a[76:79], v[204:207], v[32:35]\n"
    "v_exp_f32 v8, v8\n"
    "v_mfma_f32_16x16x32_bf16 v[36:39], a[80:83], v[192:195], v[68:71]\n"
    "ds_read_b64_tr_b16 a[106:107], v225 offset:4608\n"
    "v_cvt_pk_bf16_f32 v13, v10, v11\n"
    "v_mfma_f32_16x16x32_bf16 v[36:39], a[84:87], v[196:199], v[36:39]\n"
    "v_exp_f32 v9, v9\n"
    "v_mfma_f32_16x16x32_bf16 v[36:39], a[88:91], v[200:203], v[36:39]\n"
    "ds_read_b64_tr_b16 a[108:109], v225 offset:768\n"
    "ds_read_b64_tr_b16 a[110:111], v225 offset:4864\n"
    "v_mfma_f32_16x16x32_bf16 v[36:39], a[92:95], v[204:207], v[36:39]\n"
    "v_exp_f32 v16, v16\n"
    "v_mfma_f32_16x16x32_bf16 v[40:43], a[64:67], v[208:211], v[72:75]\n"
    "ds_read_b64_tr_b16 a[112:113], v225 offset:1024\n"
    "v_cvt_pk_bf16_f32 v12, v8, v9\n"
    "v_mfma_f32_16x16x32_bf16 v[40:43], a[68:71], v[212:215], v[40:43]\n"
    "v_exp_f32 v17, v17\n"
    "v_mfma_f32_16x16x32_bf16 v[40:43], a[72:75], v[216:219], v[40:43]\n"
    "ds_read_b64_tr_b16 a[114:115], v225 offset:5120\n"
    "v_cvt_pk_bf16_f32 v16, v16, v17\n"
    "v_mfma_f32_16x16x32_bf16 v[40:43], a[76:79], v[220:223], v[40:43]\n"
    "v_exp_f32 v18, v18\n"
    "v_mfma_f32_16x16x32_bf16 v[44:47], a[80:83], v[208:211], v[76:79]\n"
    "ds_read_b64_tr_b16 a[116:117], v225 offset:1280\n"
    "ds_read_b64_tr_b16 a[118:119], v225 offset:5376\n"
    "v_mfma_f32_16x16x32_bf16 v[44:47], a[84:87], v[212:215], v[44:47]\n"
    "v_exp_f32 v19, v19\n"
    "v_mfma_f32_16x16x32_bf16 v[44:47], a[88:91], v[216:219], v[44:47]\n"
    "ds_read_b64_tr_b16 a[120:121], v225 offset:1536\n"
    "v_cvt_pk_bf16_f32 v17, v18, v19\n"
    "v_mfma_f32_16x16x32_bf16 v[44:47], a[92:95], v[220:223], v[44:47]\n"
    "v_exp_f32 v20, v20\n"
    "v_mfma_f32_16x16x32_bf16 v[48:51], a[64:67], v[24:27], v[80:83]\n"
    "ds_read_b64_tr_b16 a[122:123], v225 offset:5632\n"
    "ds_read_b64_tr_b16 a[124:125], v225 offset:1792\n"
    "v_mfma_f32_16x16x32_bf16 v[48:51], a[68:71], v[28:31], v[48:51]\n"
    "v_exp_f32 v21, v21\n"
    "v_mfma_f32_16x16x32_bf16 v[48:51], a[72:75], v[56:59], v[48:51]\n"
    "ds_read_b64_tr_b16 a[126:127], v225 offset:5888\n"
    "v_cvt_pk_bf16_f32 v18, v20, v21\n"
    "v_mfma_f32_16x16x32_bf16 v[48:51], a[76:79], v[60:63], v[48:51]\n"
    "v_exp_f32 v22, v22\n"
    "v_mfma_f32_16x16x32_bf16 v[52:55], a[80:83], v[24:27], v[84:87]\n"
    "s_sub_u32 s56, s56, 1\n"
    "v_mfma_f32_16x16x32_bf16 v[52:55], a[84:87], v[28:31], v[52:55]\n"
    "v_exp_f32 v23, v23\n"
    "v_mfma_f32_16x16x32_bf16 v[52:55], a[88:91], v[56:59], v[52:55]\n"
    "v_cvt_pk_bf16_f32 v19, v22, v23\n"
    "v_mfma_f32_16x16x32_bf16 v[52:55], a[92:95], v[60:63], v[52:55]\n"
    "s_waitcnt lgkmcnt(0)\n"
    "s_cbranch_scc1 L3_ev0_%=\n"
    "L3_p1_%=:\n"
    "v_mfma_f32_16x16x32_bf16 v[96:99], a[96:99], v[0:3], v[96:99]\n"
    "v_exp_f32 v32, v32\n"
    "v_mfma_f32_16x16x32_bf16 v[100:103], a[100:103], v[0:3], v[100:103]\n"
    "s_add_u32 m0, s55, s59\n"
    "ds_read_b128 a[64:67], v224 offset:8192\n"
    "v_mfma_f32_16x16x32_bf16 v[104:107], a[104:107], v[0:3], v[104:107]\n"
    "v_exp_f32 v33, v33\n"
    "v_mfma_f32_16x16x32_bf16 v[108:111], a[108:111], v[0:3], v[108:111]\n"
    "ds_read_b128 a[68:71], v224 offset:8704\n"
    "v_cvt_pk_bf16_f32 v32, v32, v33\n"
    "v_mfma_f32_16x16x32_bf16 v[112:115], a[112:115], v[0:3], v[112:115]\n"
    "v_exp_f32 v34, v34\n"
    "v_mfma_f32_16x16x32_bf16 v[116:119], a[116:119], v[0:3], v[116:119]\n"
    "ds_read_b128 a[72:75], v224 offset:9216\n"
    "v_mfma_f32_16x16x32_bf16 v[120:123], a[120:123], v[0:3], v[120:123]\n"
    "s_mov_b32 s59, s58\n"
    "v_exp_f32 v35, v35\n"
    "v_mfma_f32_16x16x32_bf16 v[124:127], a[124:127], v[0:3], v[124:127]\n"
    "s_add_u32 s58, s58, 32768\n"
    "ds_read_b128 a[76:79], v224 offset:9728\n"
    "v_cvt_pk_bf16_f32 v33, v34, v35\n"
    "v_mfma_f32_16x16x32_bf16 a[128:131], a[144:147], v[0:3], a[128:131]\n"
    "s_cmp_eq_u32 s58, 98304\n"
    "s_cselect_b32 s58, 0, s58\n"
    "v_exp_f32 v36, v36\n"
    "v_mfma_f32_16x16x32_bf16 v[128:131], a[96:99], v[12:15], v[128:131]\n"
    "buffer_load_dwordx4 v232, s[40:43], s53 offen offset:0 lds\n"
    "v_mfma_f32_16x16x32_bf16 v[132:135], a[100:103], v[12:15], v[132:135]\n"
    "v_exp_f32 v37, v37\n"
    "v_mfma_f32_16x16x32_bf16 v[136:139], a[104:107], v[12:15], v[136:139]\n"
    "ds_read_b128 a[80:83], v224 offset:12288\n"
    "v_cvt_pk_bf16_f32 v34, v36, v37\n"
    "v_mfma_f32_16x16x32_bf16 v[140:143], a[108:111], v[12:15], v[140:143]\n"
    "v_exp_f32 v38, v38\n"
    "v_mfma_f32_16x16x32_bf16 v[144:147], a[112:115], v[12:15], v[144:147]\n"
    "ds_read_b128 a[84:87], v224 offset:12800\n"
    "v_mfma_f32_16x16x32_bf16 v[148:151], a[116:119], v[12:15], v[148:151]\n"
    "v_exp_f32 v39, v39\n"
    "v_mfma_f32_16x16x32_bf16 v[152:155], a[120:123], v[12:15], v[152:155]\n"
    "buffer_load_dwordx4 v233, s[40:43], s53 offen offset:1024 lds\n"
    "v_mfma_f32_16x16x32_bf16 v[156:159], a[124:127], v[12:15], v[156:159]\n"
    "v_exp_f32 v46, v46\n"
    "v_mfma_f32_16x16x32_bf16 v[160:163], a[96:99], v[16:19], v[160:163]\n"
    "ds_read_b128 a[88:91], v224 offset:13312\n"
    "v_cvt_pk_bf16_f32 v35, v38, v39\n"
    "v_mfma_f32_16x16x32_bf16 v[164:167], a[100:103], v[16:19], v[164:167]\n"
    "v_exp_f32 v47, v47\n"
    "v_mfma_f32_16x16x32_bf16 v[168:171], a[104:107], v[16:19], v[168:171]\n"
    "ds_read_b128 a[92:95], v224 offset:13824\n"
    "v_cvt_pk_bf16_f32 v47, v46, v47\n"
    "v_mfma_f32_16x16x32_bf16 v[172:175], a[108:111], v[16:19], v[172:175]\n"
    "v_exp_f32 v44, v44\n"
    "v_mfma_f32_16x16x32_bf16 v[176:179], a[112:115], v[16:19], v[176:179]\n"
    "buffer_load_dwordx4 v234, s[40:43], s53 offen offset:2048 lds\n"
    "v_mfma_f32_16x16x32_bf16 v[180:183], a[116:119], v[16:19], v[180:183]\n"
    "v_exp_f32 v45, v45\n"
    "v_mfma_f32_16x16x32_bf16 v[184:187], a[120:123], v[16:19], v[184:187]\n"
    "ds_read_b64_tr_b16 a[96:97], v225 offset:8192\n"
    "ds_read_b64_tr_b16 a[98:99], v225 offset:12288\n"
    "v_mfma_f32_16x16x32_bf16 v[188:191], a[124:127], v[16:19], v[188:191]\n"
    "buffer_load_dwordx4 v235, s[40:43], s53 offen offset:3072 lds\n"
    "v_smfmac_f32_16x16x64_bf16 a[152:155], a[192:195], v[12:19], v242\n"
    "s_waitcnt lgkmcnt(2)\n"
    "v_exp_f32 v42, v42\n"
    "v_mfma_f32_16x16x32_bf16 v[0:3], a[64:67], v[192:195], v[64:67]\n"
    "ds_read_b64_tr_b16 a[100:101], v225 offset:8448\n"
    "v_cvt_pk_bf16_f32 v46, v44, v45\n"
    "v_mfma_f32_16x16x32_bf16 v[0:3], a[68:71], v[196:199], v[0:3]\n"
    "s_add_u32 s53, s53, 0x40000\n"
    "v_exp_f32 v43, v43\n"
    "v_mfma_f32_16x16x32_bf16 v[0:3], a[72:75], v[200:203], v[0:3]\n"
    "ds_read_b64_tr_b16 a[102:103], v225 offset:12544\n"
    "ds_read_b64_tr_b16 a[104:105], v225 offset:8704\n"
    "v_mfma_f32_16x16x32_bf16 v[0:3], a[76:79], v[204:207], v[0:3]\n"
    "v_exp_f32 v40, v40\n"
    "v_mfma_f32_16x16x32_bf16 v[4:7], a[80:83], v[192:195], v[68:71]\n"
    "ds_read_b64_tr_b16 a[106:107], v225 offset:12800\n"
    "v_cvt_pk_bf16_f32 v45, v42, v43\n"
    "v_mfma_f32_16x16x32_bf16 v[4:7], a[84:87], v[196:199], v[4:7]\n"
    "v_exp_f32 v41, v41\n"
    "v_mfma_f32_16x16x32_bf16 v[4:7], a[88:91], v[200:203], v[4:7]\n"
    "ds_read_b64_tr_b16 a[108:109], v225 offset:8960\n"
    "ds_read_b64_tr_b16 a[110:111], v225 offset:13056\n"
    "v_mfma_f32_16x16x32_bf16 v[4:7], a[92:95], v[204:207], v[4:7]\n"
    "v_exp_f32 v48, v48\n"
    "v_mfma_f32_16x16x32_bf16 v[8:11], a[64:67], v[208:211], v[72:75]\n"
    "ds_read_b64_tr_b16 a[112:113], v225 offset:9216\n"
    "v_cvt_pk_bf16_f32 v44, v40, v41\n"
    "v_mfma_f32_16x16x32_bf16 v[8:11], a[68:71], v[212:215], v[8:11]\n"
    "v_exp_f32 v49, v49\n"
    "v_mfma_f32_16x16x32_bf16 v[8:11], a[72:75], v[216:219], v[8:11]\n"
    "ds_read_b64_tr_b16 a[114:115], v225 offset:13312\n"
    "v_cvt_pk_bf16_f32 v48, v48, v49\n"
    "v_mfma_f32_16x16x32_bf16 v[8:11], a[76:79], v[220:223], v[8:11]\n"
    "v_exp_f32 v50, v50\n"
    "v_mfma_f32_16x16x32_bf16 v[12:15], a[80:83], v[208:211], v[76:79]\n"
    "ds_read_b64_tr_b16 a[116:117], v225 offset:9472\n"
    "ds_read_b64_tr_b16 a[118:119], v225 offset:13568\n"
    "v_mfma_f32_16x16x32_bf16 v[12:15], a[84:87], v[212:215], v[12:15]\n"
    "v_exp_f32 v51, v51\n"
    "v_mfma_f32_16x16x32_bf16 v[12:15], a[88:91], v[216:219], v[12:15]\n"
    "ds_read_b64_tr_b16 a[120:121], v225 offset:9728\n"
    "v_cvt_pk_bf16_f32 v49, v50, v51\n"
    "v_mfma_f32_16x16x32_bf16 v[12:15], a[92:95], v[220:223], v[12:15]\n"
    "v_exp_f32 v52, v52\n"
    "v_mfma_f32_16x16x32_bf16 v[16:19], a[64:67], v[24:27], v[80:83]\n"
    "ds_read_b64_tr_b16 a[122:123], v225 offset:13824\n"
    "ds_read_b64_tr_b16 a[124:125], v225 offset:9984\n"
    "v_mfma_f32_16x16x32_bf16 v[16:19], a[68:71], v[28:31], v[16:19]\n"
    "v_exp_f32 v53, v53\n"
    "v_mfma_f32_16x16x32_bf16 v[16:19], a[72:75], v[56:59], v[16:19]\n"
    "ds_read_b64_tr_b16 a[126:127], v225 offset:14080\n"
    "v_cvt_pk_bf16_f32 v50, v52, v53\n"
    "v_mfma_f32_16x16x32_bf16 v[16:19], a[76:79], v[60:63], v[16:19]\n"
    "v_exp_f32 v54, v54\n"
    "v_mfma_f32_16x16x32_bf16 v[20:23], a[80:83], v[24:27], v[84:87]\n"
    "s_sub_u32 s56, s56, 1\n"
    "v_add_u32 v224, s58, v226\n"
    "v_mfma_f32_16x16x32_bf16 v[20:23], a[84:87], v[28:31], v[20:23]\n"
    "v_exp_f32 v55, v55\n"
    "v_mfma_f32_16x16x32_bf16 v[20:23], a[88:91], v[56:59], v[20:23]\n"
    "v_cvt_pk_bf16_f32 v51, v54, v55\n"
    "v_add_u32 v225, s58, v227\n"
    "v_mfma_f32_16x16x32_bf16 v[20:23], a[92:95], v[60:63], v[20:23]\n"
    "s_waitcnt lgkmcnt(0)\n"
    "s_cbranch_scc0 L3_p0_%=\n"
    "s_cmp_eq_u32 s57, 0\n"
    "s_cbranch_scc0 L3_fix1_%=\n"
    "L3_tail0_%=:\n"
    "v_mfma_f32_16x16x32_bf16 v[96:99], a[96:99], v[32:35], v[96:99]\n"
    "s_waitcnt vmcnt(8)\n"
    "s_barrier\n"
    "v_mfma_f32_16x16x32_bf16 v[100:103], a[100:103], v[32:35], v[100:103]\n"
    "s_mov_b32 s61, 0x5f800000\n"
    "ds_read_b64_tr_b16 a[64:65], v225 offset:0\n"
    "ds_read_b64_tr_b16 a[66:67], v225 offset:4096\n"
    "v_mfma_f32_16x16x32_bf16 v[104:107], a[104:107], v[32:35], v[104:107]\n"
    "s_mov_b64 s[76:77], 0\n"
    "v_exp_f32 v0, v0\n"
    "v_mfma_f32_16x16x32_bf16 v[108:111], a[108:111], v[32:35], v[108:111]\n"
    "ds_read_b64_tr_b16 a[68:69], v225 offset:256\n"
    "ds_read_b64_tr_b16 a[70:71], v225 offset:4352\n"
    "v_mfma_f32_16x16x32_bf16 v[112:115], a[112:115], v[32:35], v[112:115]\n"
    "v_exp_f32 v1, v1\n"
    "v_mfma_f32_16x16x32_bf16 v[116:119], a[116:119], v[32:35], v[116:119]\n"
    "ds_read_b64_tr_b16 a[72:73], v225 offset:512\n"
    "ds_read_b64_tr_b16 a[74:75], v225 offset:4608\n"
    "v_mfma_f32_16x16x32_bf16 v[120:123], a[120:123], v[32:35], v[120:123]\n"
    "v_exp_f32 v2, v2\n"
    "v_mfma_f32_16x16x32_bf16 v[124:127], a[124:127], v[32:35], v[124:127]\n"
    "ds_read_b64_tr_b16 a[76:77], v225 offset:768\n"
    "ds_read_b64_tr_b16 a[78:79], v225 offset:4864\n"
    "v_mfma_f32_16x16x32_bf16 a[128:131], a[144:147], v[32:35], a[128:131]\n"
    "v_exp_f32 v3, v3\n"
    "v_mfma_f32_16x16x32_bf16 v[128:131], a[96:99], v[44:47], v[128:131]\n"
    "ds_read_b64_tr_b16 a[80:81], v225 offset:1024\n"
    "ds_read_b64_tr_b16 a[82:83], v225 offset:5120\n"
    "v_mfma_f32_16x16x32_bf16 v[132:135], a[100:103], v[44:47], v[132:135]\n"
    "v_cvt_pk_bf16_f32 v0, v0, v1\n"
    "v_cvt_pk_bf16_f32 v1, v2, v3\n"
    "v_mfma_f32_16x16x32_bf16 v[136:139], a[104:107], v[44:47], v[136:139]\n"
    "ds_read_b64_tr_b16 a[84:85], v225 offset:1280\n"
    "ds_read_b64_tr_b16 a[86:87], v225 offset:5376\n"
    "v_mfma_f32_16x16x32_bf16 v[140:143], a[108:111], v[44:47], v[140:143]\n"
    "v_exp_f32 v4, v4\n"
    "v_mfma_f32_16x16x32_bf16 v[144:147], a[112:115], v[44:47], v[144:147]\n"
    "ds_read_b64_tr_b16 a[88:89], v225 offset:1536\n"
    "ds_read_b64_tr_b16 a[90:91], v225 offset:5632\n"
    "v_mfma_f32_16x16x32_bf16 v[148:151], a[116:119], v[44:47], v[148:151]\n"
    "v_exp_f32 v5, v5\n"
    "v_mfma_f32_16x16x32_bf16 v[152:155], a[120:123], v[44:47], v[152:155]\n"
    "ds_read_b64_tr_b16 a[92:93], v225 offset:1792\n"
    "ds_read_b64_tr_b16 a[94:95], v225 offset:5888\n"
    "v_mfma_f32_16x16x32_bf16 v[156:159], a[124:127], v[44:47], v[156:159]\n"
    "v_exp_f32 v6, v6\n"
    "v_mfma_f32_16x16x32_bf16 a[132:135], a[144:147], v[44:47], a[132:135]\n"
    "v_exp_f32 v7, v7\n"
    "v_mfma_f32_16x16x32_bf16 v[160:163], a[96:99], v[48:51], v[160:163]\n"
    "v_cvt_pk_bf16_f32 v2, v4, v5\n"
    "v_cvt_pk_bf16_f32 v3, v6, v7\n"
    "v_mfma_f32_16x16x32_bf16 v[164:167], a[100:103], v[48:51], v[164:167]\n"
    "v_exp_f32 v14, v14\n"
    "v_mfma_f32_16x16x32_bf16 v[168:171], a[104:107], v[48:51], v[168:171]\n"
    "v_exp_f32 v15, v15\n"
    "v_mfma_f32_16x16x32_bf16 v[172:175], a[108:111], v[48:51], v[172:175]\n"
    "v_exp_f32 v12, v12\n"
    "v_mfma_f32_16x16x32_bf16 v[176:179], a[112:115], v[48:51], v[176:179]\n"
    "v_exp_f32 v13, v13\n"
    "v_mfma_f32_16x16x32_bf16 v[180:183], a[116:119], v[48:51], v[180:183]\n"
    "v_cvt_pk_bf16_f32 v15, v14, v15\n"
    "v_cvt_pk_bf16_f32 v14, v12, v13\n"
    "v_mfma_f32_16x16x32_bf16 v[184:187], a[120:123], v[48:51], v[184:187]\n"
    "v_exp_f32 v10, v10\n"
    "v_mfma_f32_16x16x32_bf16 v[188:191], a[124:127], v[48:51], v[188:191]\n"
    "v_exp_f32 v11, v11\n"
    "v_mfma_f32_16x16x32_bf16 a[136:139], a[144:147], v[48:51], a[136:139]\n"
    "v_exp_f32 v8, v8\n"
    "s_waitcnt lgkmcnt(0)\n"
    "v_mfma_f32_16x16x32_bf16 a[128:131], a[144:147], v[0:3], a[128:131]\n"
    "v_exp_f32 v9, v9\n"
    "v_mfma_f32_16x16x32_bf16 v[96:99], a[64:67], v[0:3], v[96:99]\n"
    "v_cvt_pk_bf16_f32 v13, v10, v11\n"
    "v_cvt_pk_bf16_f32 v12, v8, v9\n"
    "v_mfma_f32_16x16x32_bf16 v[100:103], a[68:71], v[0:3], v[100:103]\n"
    "v_exp_f32 v16, v16\n"
    "v_mfma_f32_16x16x32_bf16 v[104:107], a[72:75], v[0:3], v[104:107]\n"
    "v_exp_f32 v17, v17\n"
    "v_mfma_f32_16x16x32_bf16 v[108:111], a[76:79], v[0:3], v[108:111]\n"
    "v_exp_f32 v18, v18\n"
    "v_mfma_f32_16x16x32_bf16 v[112:115], a[80:83], v[0:3], v[112:115]\n"
    "v_exp_f32 v19, v19\n"
    "v_mfma_f32_16x16x32_bf16 v[116:119], a[84:87], v[0:3], v[116:119]\n"
    "v_cvt_pk_bf16_f32 v16, v16, v17\n"
    "v_cvt_pk_bf16_f32 v17, v18, v19\n"
    "v_mfma_f32_16x16x32_bf16 v[120:123], a[88:91], v[0:3], v[120:123]\n"
    "v_exp_f32 v20, v20\n"
    "v_mfma_f32_16x16x32_bf16 v[124:127], a[92:95], v[0:3], v[124:127]\n"
    "v_exp_f32 v21, v21\n"
    "v_mfma_f32_16x16x32_bf16 a[132:135], a[144:147], v[12:15], a[132:135]\n"
    "v_exp_f32 v22, v22\n"
    "v_mfma_f32_16x16x32_bf16 v[128:131], a[64:67], v[12:15], v[128:131]\n"
    "v_exp_f32 v23, v23\n"
    "v_mfma_f32_16x16x32_bf16 v[132:135], a[68:71], v[12:15], v[132:135]\n"
    "v_cvt_pk_bf16_f32 v18, v20, v21\n"
    "v_cvt_pk_bf16_f32 v19, v22, v23\n"
    "v_mfma_f32_16x16x32_bf16 v[136:139], a[72:75], v[12:15], v[136:139]\n"
    "v_accvgpr_read_b32 v88, a128\n"
    "v_cmp_nge_f32 vcc, s61, v88\n"
    "v_mfma_f32_16x16x32_bf16 v[140:143], a[76:79], v[12:15], v[140:143]\n"
    "v_rcp_f32 v64, v88\n"
    "v_mfma_f32_16x16x32_bf16 v[144:147], a[80:83], v[12:15], v[144:147]\n"
    "s_or_b64 s[76:77], s[76:77], vcc\n"
    "v_mov_b32 v65, v64\n"
    "v_mfma_f32_16x16x32_bf16 v[148:151], a[84:87], v[12:15], v[148:151]\n"
    "v_mul_f32 v96, v96, v64\n"
    "v_mul_f32 v97, v97, v64\n"
    "v_mfma_f32_16x16x32_bf16 v[152:155], a[88:91], v[12:15], v[152:155]\n"
    "v_mul_f32 v98, v98, v64\n"
    "v_mul_f32 v99, v99, v64\n"
    "v_mfma_f32_16x16x32_bf16 v[156:159], a[92:95], v[12:15], v[156:159]\n"
    "v_cvt_pk_bf16_f32 v96, v96, v97\n"
    "v_cvt_pk_bf16_f32 v97, v98, v99\n"
    "v_mfma_f32_16x16x32_bf16 a[136:139], a[144:147], v[16:19], a[136:139]\n"
    "ds_write_b64 v251, v[96:97] offset:0\n"
    "v_mul_f32 v100, v100, v64\n"
    "v_mfma_f32_16x16x32_bf16 v[160:163], a[64:67], v[16:19], v[160:163]\n"
    "v_mul_f32 v101, v101, v64\n"
    "v_mul_f32 v102, v102, v64\n"
    "v_mfma_f32_16x16x32_bf16 v[164:167], a[68:71], v[16:19], v[164:167]\n"
    "v_mul_f32 v103, v103, v64\n"
    "v_cvt_pk_bf16_f32 v100, v100, v101\n"
    "v_mfma_f32_16x16x32_bf16 v[168:171], a[72:75], v[16:19], v[168:171]\n"
    "v_cvt_pk_bf16_f32 v101, v102, v103\n"
    "v_mul_f32 v104, v104, v64\n"
    "v_mfma_f32_16x16x32_bf16 v[172:175], a[76:79], v[16:19], v[172:175]\n"
    "ds_write_b64 v251, v[100:101] offset:32\n"
    "v_mul_f32 v105, v105, v64\n"
    "v_mfma_f32_16x16x32_bf16 v[176:179], a[80:83], v[16:19], v[176:179]\n"
    "v_mul_f32 v106, v106, v64\n"
    "v_mul_f32 v107, v107, v64\n"
    "v_mfma_f32_16x16x32_bf16 v[180:183], a[84:87], v[16:19], v[180:183]\n"
    "v_cvt_pk_bf16_f32 v104, v104, v105\n"
    "v_cvt_pk_bf16_f32 v105, v106, v107\n"
    "v_mfma_f32_16x16x32_bf16 v[184:187], a[88:91], v[16:19], v[184:187]\n"
    "ds_write_b64 v251, v[104:105] offset:64\n"
    "v_mul_f32 v108, v108, v64\n"
    "v_mfma_f32_16x16x32_bf16 v[188:191], a[92:95], v[16:19], v[188:191]\n"
    "v_mul_f32 v109, v109, v64\n"
    "v_mul_f32 v110, v110, v64\n"
    "v_mul_f32 v111, v111, v64\n"
    "v_cvt_pk_bf16_f32 v108, v108, v109\n"
    "v_cvt_pk_bf16_f32 v109, v110, v111\n"
    "v_pk_mul_f32 v[112:113], v[112:113], v[64:65]\n"
    "v_pk_mul_f32 v[114:115], v[114:115], v[64:65]\n"
    "v_cvt_pk_bf16_f32 v112, v112, v113\n"
    "v_cvt_pk_bf16_f32 v113, v114, v115\n"
    "v_pk_mul_f32 v[116:117], v[116:117], v[64:65]\n"
    "v_pk_mul_f32 v[118:119], v[118:119], v[64:65]\n"
    "v_cvt_pk_bf16_f32 v116, v116, v117\n"
    "v_cvt_pk_bf16_f32 v117, v118, v119\n"
    "ds_write_b64 v251, v[108:109] offset:96\n"
    "ds_write_b64 v251, v[112:113] offset:128\n"
    "ds_write_b64 v251, v[116:117] offset:160\n"
    "v_pk_mul_f32 v[120:121], v[120:121], v[64:65]\n"
    "v_pk_mul_f32 v[122:123], v[122:123], v[64:65]\n"
    "v_cvt_pk_bf16_f32 v120, v120, v121\n"
    "v_cvt_pk_bf16_f32 v121, v122, v123\n"
    "v_pk_mul_f32 v[124:125], v[124:125], v[64:65]\n"
    "v_pk_mul_f32 v[126:127], v[126:127], v[64:65]\n"
    "v_cvt_pk_bf16_f32 v124, v124, v125\n"
    "v_cvt_pk_bf16_f32 v125, v126, v127\n"
    "ds_write_b64 v251, v[120:121] offset:192\n"
    "ds_write_b64 v251, v[124:125] offset:224\n"
    "ds_read_b128 v[32:35], v252 offset:0\n"
    "ds_read_b128 v[36:39], v252 offset:1088\n"
    "ds_read_b128 v[40:43], v252 offset:2176\n"
    "ds_read_b128 v[44:47], v252 offset:3264\n"
    "v_accvgpr_read_b32 v89, a132\n"
    "v_accvgpr_read_b32 v66, a152\n"
    "v_accvgpr_read_b32 v67, a153\n"
    "v_add_f32 v89, v89, v66\n"
    "v_add_f32 v89, v89, v67\n"
    "v_cmp_nge_f32 vcc, s61, v89\n"
    "v_rcp_f32 v66, v89\n"
    "s_or_b64 s[76:77], s[76:77], vcc\n"
    "v_mov_b32 v67, v66\n"
    "v_pk_mul_f32 v[128:129], v[128:129], v[66:67]\n"
    "v_pk_mul_f32 v[130:131], v[130:131], v[66:67]\n"
    "v_cvt_pk_bf16_f32 v128, v128, v129\n"
    "v_cvt_pk_bf16_f32 v129, v130, v131\n"
    "v_pk_mul_f32 v[132:133], v[132:133], v[66:67]\n"
    "v_pk_mul_f32 v[134:135], v[134:135], v[66:67]\n"
    "v_cvt_pk_bf16_f32 v132, v132, v133\n"
    "v_cvt_pk_bf16_f32 v133, v134, v135\n"
    "s_mov_b32 s53, 0\n"
    "s_waitcnt lgkmcnt(3)\n"
    "buffer_store_dwordx4 v[32:35], v253, s[48:51], s53 offen nt\n"
    "ds_write_b64 v251, v[128:129] offset:4352\n"
    "ds_write_b64 v251, v[132:133] offset:4384\n"
    "v_pk_mul_f32 v[136:137], v[136:137], v[66:67]\n"
    "v_pk_mul_f32 v[138:139], v[138:139], v[66:67]\n"
    "v_cvt_pk_bf16_f32 v136, v136, v137\n"
    "v_cvt_pk_bf16_f32 v137, v138, v139\n"
    "v_pk_mul_f32 v[140:141], v[140:141], v[66:67]\n"
    "v_pk_mul_f32 v[142:143], v[142:143], v[66:67]\n"
    "v_cvt_pk_bf16_f32 v140, v140, v141\n"
    "v_cvt_pk_bf16_f32 v141, v142, v143\n"
    "s_mov_b32 s53, 16384\n"
    "s_waitcnt lgkmcnt(4)\n"
    "buffer_store_dwordx4 v[36:39], v253, s[48:51], s53 offen nt\n"
    "ds_write_b64 v251, v[136:137] offset:4416\n"
    "ds_write_b64 v251, v[140:141] offset:4448\n"
    "v_pk_mul_f32 v[144:145], v[144:145], v[66:67]\n"
    "v_pk_mul_f32 v[146:147], v[146:147], v[66:67]\n"
    "v_cvt_pk_bf16_f32 v144, v144, v145\n"
    "v_cvt_pk_bf16_f32 v145, v146, v147\n"
    "v_pk_mul_f32 v[148:149], v[148:149], v[66:67]\n"
    "v_pk_mul_f32 v[150:151], v[150:151], v[66:67]\n"
    "v_cvt_pk_bf16_f32 v148, v148, v149\n"
    "v_cvt_pk_bf16_f32 v149, v150, v151\n"
    "s_mov_b32 s53, 32768\n"
    "s_waitcnt lgkmcnt(5)\n"
    "buffer_store_dwordx4 v[40:43], v253, s[48:51], s53 offen nt\n"
    "ds_write_b64 v251, v[144:145] offset:4480\n"
    "ds_write_b64 v251, v[148:149] offset:4512\n"
    "v_pk_mul_f32 v[152:153], v[152:153], v[66:67]\n"
    "v_pk_mul_f32 v[154:155], v[154:155], v[66:67]\n"
    "v_cvt_pk_bf16_f32 v152, v152, v153\n"
    "v_cvt_pk_bf16_f32 v153, v154, v155\n"
    "v_pk_mul_f32 v[156:157], v[156:157], v[66:67]\n"
    "v_pk_mul_f32 v[158:159], v[158:159], v[66:67]\n"
    "v_cvt_pk_bf16_f32 v156, v156, v157\n"
    "v_cvt_pk_bf16_f32 v157, v158, v159\n"
    "s_mov_b32 s53, 49152\n"
    "s_waitcnt lgkmcnt(6)\n"
    "buffer_store_dwordx4 v[44:47], v253, s[48:51], s53 offen nt\n"
    "ds_write_b64 v251, v[152:153] offset:4544\n"
    "ds_write_b64 v251, v[156:157] offset:4576\n"
    "ds_read_b128 v[48:51], v252 offset:4352\n"
    "ds_read_b128 v[52:55], v252 offset:5440\n"
    "ds_read_b128 v[56:59], v252 offset:6528\n"
    "ds_read_b128 v[60:63], v252 offset:7616\n"
    "v_accvgpr_read_b32 v90, a136\n"
    "v_accvgpr_read_b32 v68, a154\n"
    "v_accvgpr_read_b32 v69, a155\n"
    "v_add_f32 v90, v90, v68\n"
    "v_add_f32 v90, v90, v69\n"
    "v_cmp_nge_f32 vcc, s61, v90\n"
    "v_rcp_f32 v68, v90\n"
    "s_or_b64 s[76:77], s[76:77], vcc\n"
    "v_mov_b32 v69, v68\n"
    "v_pk_mul_f32 v[160:161], v[160:161], v[68:69]\n"
    "v_pk_mul_f32 v[162:163], v[162:163], v[68:69]\n"
    "v_cvt_pk_bf16_f32 v160, v160, v161\n"
    "v_cvt_pk_bf16_f32 v161, v162, v163\n"
    "v_pk_mul_f32 v[164:165], v[164:165], v[68:69]\n"
    "v_pk_mul_f32 v[166:167], v[166:167], v[68:69]\n"
    "v_cvt_pk_bf16_f32 v164, v164, v165\n"
    "v_cvt_pk_bf16_f32 v165, v166, v167\n"
    "s_mov_b32 s53, 65536\n"
    "s_waitcnt lgkmcnt(3)\n"
    "buffer_store_dwordx4 v[48:51], v253, s[48:51], s53 offen nt\n"
    "ds_write_b64 v251, v[160:161] offset:8704\n"
    "ds_write_b64 v251, v[164:165] offset:8736\n"
    "v_pk_mul_f32 v[168:169], v[168:169], v[68:69]\n"
    "v_pk_mul_f32 v[170:171], v[170:171], v[68:69]\n"
    "v_cvt_pk_bf16_f32 v168, v168, v169\n"
    "v_cvt_pk_bf16_f32 v169, v170, v171\n"
    "v_pk_mul_f32 v[172:173], v[172:173], v[68:69]\n"
    "v_pk_mul_f32 v[174:175], v[174:175], v[68:69]\n"
    "v_cvt_pk_bf16_f32 v172, v172, v173\n"
    "v_cvt_pk_bf16_f32 v173, v174, v175\n"
    "s_mov_b32 s53, 81920\n"
    "s_waitcnt lgkmcnt(4)\n"
    "buffer_store_dwordx4 v[52:55], v253, s[48:51], s53 offen nt\n"
    "ds_write_b64 v251, v[168:169] offset:8768\n"
    "ds_write_b64 v251, v[172:173] offset:8800\n"
    "v_pk_mul_f32 v[176:177], v[176:177], v[68:69]\n"
    "v_pk_mul_f32 v[178:179], v[178:179], v[68:69]\n"
    "v_cvt_pk_bf16_f32 v176, v176, v177\n"
    "v_cvt_pk_bf16_f32 v177, v178, v179\n"
    "v_pk_mul_f32 v[180:181], v[180:181], v[68:69]\n"
    "v_pk_mul_f32 v[182:183], v[182:183], v[68:69]\n"
    "v_cvt_pk_bf16_f32 v180, v180, v181\n"
    "v_cvt_pk_bf16_f32 v181, v182, v183\n"
    "s_mov_b32 s53, 98304\n"
    "s_waitcnt lgkmcnt(5)\n"
    "buffer_store_dwordx4 v[56:59], v253, s[48:51], s53 offen nt\n"
    "ds_write_b64 v251, v[176:177] offset:8832\n"
    "ds_write_b64 v251, v[180:181] offset:8864\n"
    "v_pk_mul_f32 v[184:185], v[184:185], v[68:69]\n"
    "v_pk_mul_f32 v[186:187], v[186:187], v[68:69]\n"
    "v_cvt_pk_bf16_f32 v184, v184, v185\n"
    "v_cvt_pk_bf16_f32 v185, v186, v187\n"
    "v_pk_mul_f32 v[188:189], v[188:189], v[68:69]\n"
    "v_pk_mul_f32 v[190:191], v[190:191], v[68:69]\n"
    "v_cvt_pk_bf16_f32 v188, v188, v189\n"
    "v_cvt_pk_bf16_f32 v189, v190, v191\n"
    "s_mov_b32 s53, 114688\n"
    "s_waitcnt lgkmcnt(6)\n"
    "buffer_store_dwordx4 v[60:63], v253, s[48:51], s53 offen nt\n"
    "ds_write_b64 v251, v[184:185] offset:8896\n"
    "ds_write_b64 v251, v[188:189] offset:8928\n"
    "ds_read_b128 v[32:35], v252 offset:8704\n"
    "ds_read_b128 v[36:39], v252 offset:9792\n"
    "ds_read_b128 v[40:43], v252 offset:10880\n"
    "ds_read_b128 v[44:47], v252 offset:11968\n"
    "s_mov_b32 s53, 131072\n"
    "s_waitcnt lgkmcnt(3)\n"
    "buffer_store_dwordx4 v[32:35], v253, s[48:51], s53 offen nt\n"
    "s_mov_b32 s53, 147456\n"
    "s_waitcnt lgkmcnt(2)\n"
    "buffer_store_dwordx4 v[36:39], v253, s[48:51], s53 offen nt\n"
    "s_mov_b32 s53, 163840\n"
    "s_waitcnt lgkmcnt(1)\n"
    "buffer_store_dwordx4 v[40:43], v253, s[48:51], s53 offen nt\n"
    "s_mov_b32 s53, 180224\n"
    "s_waitcnt lgkmcnt(0)\n"
    "buffer_store_dwordx4 v[44:47], v253, s[48:51], s53 offen nt\n"
    "s_or_b32 %0, s76, s77\n"
    "s_branch L3_end_%=\n"
    "L3_m0_0x_%=:\n"
    "s_nop 7\n"
    "v_cmp_ge_i32 s[68:69], 0, v250\n"
    "v_cmp_ge_i32 s[70:71], 1, v250\n"
    "v_cmp_ge_i32 s[72:73], 2, v250\n"
    "v_cmp_ge_i32 s[74:75], 3, v250\n"
    "s_nop 1\n"
    "v_cndmask_b32 v0, v0, v249, s[68:69]\n"
    "v_cndmask_b32 v1, v1, v249, s[70:71]\n"
    "v_cndmask_b32 v2, v2, v249, s[72:73]\n"
    "v_cndmask_b32 v3, v3, v249, s[74:75]\n"
    "v_cmp_ge_i32 s[68:69], 16, v250\n"
    "v_cmp_ge_i32 s[70:71], 17, v250\n"
    "v_cmp_ge_i32 s[72:73], 18, v250\n"
    "v_cmp_ge_i32 s[74:75], 19, v250\n"
    "s_nop 1\n"
    "v_cndmask_b32 v4, v4, v249, s[68:69]\n"
    "v_cndmask_b32 v5, v5, v249, s[70:71]\n"
    "v_cndmask_b32 v6, v6, v249, s[72:73]\n"
    "v_cndmask_b32 v7, v7, v249, s[74:75]\n"
    "s_branch L3_m0_0_%=\n"
    "L3_m1_0x_%=:\n"
    "v_cmp_ge_i32 s[68:69], 0, v248\n"
    "v_cmp_ge_i32 s[70:71], 1, v248\n"
    "v_cmp_ge_i32 s[72:73], 2, v248\n"
    "v_cmp_ge_i32 s[74:75], 3, v248\n"
    "s_nop 1\n"
    "v_cndmask_b32 v64, v64, v249, s[68:69]\n"
    "v_cndmask_b32 v65, v65, v249, s[70:71]\n"
    "v_cndmask_b32 v66, v66, v249, s[72:73]\n"
    "v_cndmask_b32 v67, v67, v249, s[74:75]\n"
    "v_cmp_ge_i32 s[68:69], 16, v248\n"
    "v_cmp_ge_i32 s[70:71], 17, v248\n"
    "v_cmp_ge_i32 s[72:73], 18, v248\n"
    "v_cmp_ge_i32 s[74:75], 19, v248\n"
    "s_nop 1\n"
    "v_cndmask_b32 v68, v68, v249, s[68:69]\n"
    "v_cndmask_b32 v69, v69, v249, s[70:71]\n"
    "v_cndmask_b32 v70, v70, v249, s[72:73]\n"
    "v_cndmask_b32 v71, v71, v249, s[74:75]\n"
    "s_branch L3_m1_0_%=\n"
    "L3_m0_1x_%=:\n"
    "s_nop 7\n"
    "v_cmp_ge_i32 s[68:69], 0, v250\n"
    "v_cmp_ge_i32 s[70:71], 1, v250\n"
    "v_cmp_ge_i32 s[72:73], 2, v250\n"
    "v_cmp_ge_i32 s[74:75], 3, v250\n"
    "s_nop 1\n"
    "v_cndmask_b32 v8, v8, v249, s[68:69]\n"
    "v_cndmask_b32 v9, v9, v249, s[70:71]\n"
    "v_cndmask_b32 v10, v10, v249, s[72:73]\n"
    "v_cndmask_b32 v11, v11, v249, s[74:75]\n"
    "v_cmp_ge_i32 s[68:69], 16, v250\n"
    "v_cmp_ge_i32 s[70:71], 17, v250\n"
    "v_cmp_ge_i32 s[72:73], 18, v250\n"
    "v_cmp_ge_i32 s[74:75], 19, v250\n"
    "s_nop 1\n"
    "v_cndmask_b32 v12, v12, v249, s[68:69]\n"
    "v_cndmask_b32 v13, v13, v249, s[70:71]\n"
    "v_cndmask_b32 v14, v14, v249, s[72:73]\n"
    "v_cndmask_b32 v15, v15, v249, s[74:75]\n"
    "s_branch L3_m0_1_%=\n"
    "L3_m1_1x_%=:\n"
    "v_cmp_ge_i32 s[68:69], 0, v248\n"
    "v_cmp_ge_i32 s[70:71], 1, v248\n"
    "v_cmp_ge_i32 s[72:73], 2, v248\n"
    "v_cmp_ge_i32 s[74:75], 3, v248\n"
    "s_nop 1\n"
    "v_cndmask_b32 v72, v72, v249, s[68:69]\n"
    "v_cndmask_b32 v73, v73, v249, s[70:71]\n"
    "v_cndmask_b32 v74, v74, v249, s[72:73]\n"
    "v_cndmask_b32 v75, v75, v249, s[74:75]\n"
    "v_cmp_ge_i32 s[68:69], 16, v248\n"
    "v_cmp_ge_i32 s[70:71], 17, v248\n"
    "v_cmp_ge_i32 s[72:73], 18, v248\n"
    "v_cmp_ge_i32 s[74:75], 19, v248\n"
    "s_nop 1\n"
    "v_cndmask_b32 v76, v76, v249, s[68:69]\n"
    "v_cndmask_b32 v77, v77, v249, s[70:71]\n"
    "v_cndmask_b32 v78, v78, v249, s[72:73]\n"
    "v_cndmask_b32 v79, v79, v249, s[74:75]\n"
    "s_branch L3_m1_1_%=\n"
    "L3_m0_2x_%=:\n"
    "s_nop 7\n"
    "v_cmp_ge_i32 s[68:69], 0, v250\n"
    "v_cmp_ge_i32 s[70:71], 1, v250\n"
    "v_cmp_ge_i32 s[72:73], 2, v250\n"
    "v_cmp_ge_i32 s[74:75], 3, v250\n"
    "s_nop 1\n"
    "v_cndmask_b32 v16, v16, v249, s[68:69]\n"
    "v_cndmask_b32 v17, v17, v249, s[70:71]\n"
    "v_cndmask_b32 v18, v18, v249, s[72:73]\n"
    "v_cndmask_b32 v19, v19, v249, s[74:75]\n"
    "v_cmp_ge_i32 s[68:69], 16, v250\n"
    "v_cmp_ge_i32 s[70:71], 17, v250\n"
    "v_cmp_ge_i32 s[72:73], 18, v250\n"
    "v_cmp_ge_i32 s[74:75], 19, v250\n"
    "s_nop 1\n"
    "v_cndmask_b32 v20, v20, v249, s[68:69]\n"
    "v_cndmask_b32 v21, v21, v249, s[70:71]\n"
    "v_cndmask_b32 v22, v22, v249, s[72:73]\n"
    "v_cndmask_b32 v23, v23, v249, s[74:75]\n"
    "s_branch L3_m0_2_%=\n"
    "L3_m1_2x_%=:\n"
    "v_cmp_ge_i32 s[68:69], 0, v248\n"
    "v_cmp_ge_i32 s[70:71], 1, v248\n"
    "v_cmp_ge_i32 s[72:73], 2, v248\n"
    "v_cmp_ge_i32 s[74:75], 3, v248\n"
    "s_nop 1\n"
    "v_cndmask_b32 v80, v80, v249, s[68:69]\n"
    "v_cndmask_b32 v81, v81, v249, s[70:71]\n"
    "v_cndmask_b32 v82, v82, v249, s[72:73]\n"
    "v_cndmask_b32 v83, v83, v249, s[74:75]\n"
    "v_cmp_ge_i32 s[68:69], 16, v248\n"
    "v_cmp_ge_i32 s[70:71], 17, v248\n"
    "v_cmp_ge_i32 s[72:73], 18, v248\n"
    "v_cmp_ge_i32 s[74:75], 19, v248\n"
    "s_nop 1\n"
    "v_cndmask_b32 v84, v84, v249, s[68:69]\n"
    "v_cndmask_b32 v85, v85, v249, s[70:71]\n"
    "v_cndmask_b32 v86, v86, v249, s[72:73]\n"
    "v_cndmask_b32 v87, v87, v249, s[74:75]\n"
    "s_branch L3_m1_2_%=\n"
    "L3_fix0_%=:\n"
    "v_cmp_ge_i32 s[68:69], 0, v248\n"
    "v_cmp_ge_i32 s[70:71], 1, v248\n"
    "v_cmp_ge_i32 s[72:73], 2, v248\n"
    "v_cmp_ge_i32 s[74:75], 3, v248\n"
    "s_nop 1\n"
    "v_cndmask_b32 v64, v64, v249, s[68:69]\n"
    "v_cndmask_b32 v72, v72, v249, s[68:69]\n"
    "v_cndmask_b32 v80, v80, v249, s[68:69]\n"
    "v_cndmask_b32 v65, v65, v249, s[70:71]\n"
    "v_cndmask_b32 v73, v73, v249, s[70:71]\n"
    "v_cndmask_b32 v81, v81, v249, s[70:71]\n"
    "v_cndmask_b32 v66, v66, v249, s[72:73]\n"
    "v_cndmask_b32 v74, v74, v249, s[72:73]\n"
    "v_cndmask_b32 v82, v82, v249, s[72:73]\n"
    "v_cndmask_b32 v67, v67, v249, s[74:75]\n"
    "v_cndmask_b32 v75, v75, v249, s[74:75]\n"
    "v_cndmask_b32 v83, v83, v249, s[74:75]\n"
    "v_cmp_ge_i32 s[68:69], 16, v248\n"
    "v_cmp_ge_i32 s[70:71], 17, v248\n"
    "v_cmp_ge_i32 s[72:73], 18, v248\n"
    "v_cmp_ge_i32 s[74:75], 19, v248\n"
    "s_nop 1\n"
    "v_cndmask_b32 v68, v68, v249, s[68:69]\n"
    "v_cndmask_b32 v76, v76, v249, s[68:69]\n"
    "v_cndmask_b32 v84, v84, v249, s[68:69]\n"
    "v_cndmask_b32 v69, v69, v249, s[70:71]\n"
    "v_cndmask_b32 v77, v77, v249, s[70:71]\n"
    "v_cndmask_b32 v85, v85, v249, s[70:71]\n"
    "v_cndmask_b32 v70, v70, v249, s[72:73]\n"
    "v_cndmask_b32 v78, v78, v249, s[72:73]\n"
    "v_cndmask_b32 v86, v86, v249, s[72:73]\n"
    "v_cndmask_b32 v71, v71, v249, s[74:75]\n"
    "v_cndmask_b32 v79, v79, v249, s[74:75]\n"
    "v_cndmask_b32 v87, v87, v249, s[74:75]\n"
    "s_mov_b32 s57, 0\n"
    "s_mov_b32 s56, 0\n"
    "s_branch L3_p1_%=\n"
    "L3_fix1_%=:\n"
    "v_cmp_ge_i32 s[68:69], 0, v248\n"
    "v_cmp_ge_i32 s[70:71], 1, v248\n"
    "v_cmp_ge_i32 s[72:73], 2, v248\n"
    "v_cmp_ge_i32 s[74:75], 3, v248\n"
    "s_nop 1\n"
    "v_cndmask_b32 v64, v64, v249, s[68:69]\n"
    "v_cndmask_b32 v72, v72, v249, s[68:69]\n"
    "v_cndmask_b32 v80, v80, v249, s[68:69]\n"
    "v_cndmask_b32 v65, v65, v249, s[70:71]\n"
    "v_cndmask_b32 v73, v73, v249, s[70:71]\n"
    "v_cndmask_b32 v81, v81, v249, s[70:71]\n"
    "v_cndmask_b32 v66, v66, v249, s[72:73]\n"
    "v_cndmask_b32 v74, v74, v249, s[72:73]\n"
    "v_cndmask_b32 v82, v82, v249, s[72:73]\n"
    "v_cndmask_b32 v67, v67, v249, s[74:75]\n"
    "v_cndmask_b32 v75, v75, v249, s[74:75]\n"
    "v_cndmask_b32 v83, v83, v249, s[74:75]\n"
    "v_cmp_ge_i32 s[68:69], 16, v248\n"
    "v_cmp_ge_i32 s[70:71], 17, v248\n"
    "v_cmp_ge_i32 s[72:73], 18, v248\n"
    "v_cmp_ge_i32 s[74:75], 19, v248\n"
    "s_nop 1\n"
    "v_cndmask_b32 v68, v68, v249, s[68:69]\n"
    "v_cndmask_b32 v76, v76, v249, s[68:69]\n"
    "v_cndmask_b32 v84, v84, v249, s[68:69]\n"
    "v_cndmask_b32 v69, v69, v249, s[70:71]\n"
    "v_cndmask_b32 v77, v77, v249, s[70:71]\n"
    "v_cndmask_b32 v85, v85, v249, s[70:71]\n"
    "v_cndmask_b32 v70, v70, v249, s[72:73]\n"
    "v_cndmask_b32 v78, v78, v249, s[72:73]\n"
    "v_cndmask_b32 v86, v86, v249, s[72:73]\n"
    "v_cndmask_b32 v71, v71, v249, s[74:75]\n"
    "v_cndmask_b32 v79, v79, v249, s[74:75]\n"
    "v_cndmask_b32 v87, v87, v249, s[74:75]\n"
    "s_mov_b32 s57, 0\n"
    "s_mov_b32 s56, 0\n"
    "s_branch L3_p0_%=\n"
    "L3_ev0_%=:\n"
    "s_cmp_eq_u32 s57, 0\n"
    "s_cbranch_scc0 L3_fix0_%=\n"
    "v_mfma_f32_16x16x32_bf16 v[96:99], a[96:99], v[0:3], v[96:99]\n"
    "s_mov_b32 s61, 0x5f800000\n"
    "ds_read_b64_tr_b16 a[64:65], v225 offset:8192\n"
    "ds_read_b64_tr_b16 a[66:67], v225 offset:12288\n"
    "v_mfma_f32_16x16x32_bf16 v[100:103], a[100:103], v[0:3], v[100:103]\n"
    "s_mov_b64 s[76:77], 0\n"
    "v_exp_f32 v32, v32\n"
    "v_mfma_f32_16x16x32_bf16 v[104:107], a[104:107], v[0:3], v[104:107]\n"
    "ds_read_b64_tr_b16 a[68:69], v225 offset:8448\n"
    "ds_read_b64_tr_b16 a[70:71], v225 offset:12544\n"
    "v_mfma_f32_16x16x32_bf16 v[108:111], a[108:111], v[0:3], v[108:111]\n"
    "v_exp_f32 v33, v33\n"
    "v_mfma_f32_16x16x32_bf16 v[112:115], a[112:115], v[0:3], v[112:115]\n"
    "ds_read_b64_tr_b16 a[72:73], v225 offset:8704\n"
    "ds_read_b64_tr_b16 a[74:75], v225 offset:12800\n"
    "v_mfma_f32_16x16x32_bf16 v[116:119], a[116:119], v[0:3], v[116:119]\n"
    "v_exp_f32 v34, v34\n"
    "v_mfma_f32_16x16x32_bf16 v[120:123], a[120:123], v[0:3], v[120:123]\n"
    "ds_read_b64_tr_b16 a[76:77], v225 offset:8960\n"
    "ds_read_b64_tr_b16 a[78:79], v225 offset:13056\n"
    "v_mfma_f32_16x16x32_bf16 v[124:127], a[124:127], v[0:3], v[124:127]\n"
    "v_exp_f32 v35, v35\n"
    "v_mfma_f32_16x16x32_bf16 a[128:131], a[144:147], v[0:3], a[128:131]\n"
    "ds_read_b64_tr_b16 a[80:81], v225 offset:9216\n"
    "ds_read_b64_tr_b16 a[82:83], v225 offset:13312\n"
    "v_mfma_f32_16x16x32_bf16 v[128:131], a[96:99], v[12:15], v[128:131]\n"
    "v_cvt_pk_bf16_f32 v32, v32, v33\n"
    "v_cvt_pk_bf16_f32 v33, v34, v35\n"
    "v_mfma_f32_16x16x32_bf16 v[132:135], a[100:103], v[12:15], v[132:135]\n"
    "ds_read_b64_tr_b16 a[84:85], v225 offset:9472\n"
    "ds_read_b64_tr_b16 a[86:87], v225 offset:13568\n"
    "v_mfma_f32_16x16x32_bf16 v[136:139], a[104:107], v[12:15], v[136:139]\n"
    "v_exp_f32 v36, v36\n"
    "v_mfma_f32_16x16x32_bf16 v[140:143], a[108:111], v[12:15], v[140:143]\n"
    "ds_read_b64_tr_b16 a[88:89], v225 offset:9728\n"
    "ds_read_b64_tr_b16 a[90:91], v225 offset:13824\n"
    "v_mfma_f32_16x16x32_bf16 v[144:147], a[112:115], v[12:15], v[144:147]\n"
    "v_exp_f32 v37, v37\n"
    "v_mfma_f32_16x16x32_bf16 v[148:151], a[116:119], v[12:15], v[148:151]\n"
    "ds_read_b64_tr_b16 a[92:93], v225 offset:9984\n"
    "ds_read_b64_tr_b16 a[94:95], v225 offset:14080\n"
    "v_mfma_f32_16x16x32_bf16 v[152:155], a[120:123], v[12:15], v[152:155]\n"
    "v_exp_f32 v38, v38\n"
    "v_mfma_f32_16x16x32_bf16 v[156:159], a[124:127], v[12:15], v[156:159]\n"
    "v_exp_f32 v39, v39\n"
    "v_mfma_f32_16x16x32_bf16 a[132:135], a[144:147], v[12:15], a[132:135]\n"
    "v_cvt_pk_bf16_f32 v34, v36, v37\n"
    "v_cvt_pk_bf16_f32 v35, v38, v39\n"
    "v_mfma_f32_16x16x32_bf16 v[160:163], a[96:99], v[16:19], v[160:163]\n"
    "v_exp_f32 v46, v46\n"
    "v_mfma_f32_16x16x32_bf16 v[164:167], a[100:103], v[16:19], v[164:167]\n"
    "v_exp_f32 v47, v47\n"
    "v_mfma_f32_16x16x32_bf16 v[168:171], a[104:107], v[16:19], v[168:171]\n"
    "v_exp_f32 v44, v44\n"
    "v_mfma_f32_16x16x32_bf16 v[172:175], a[108:111], v[16:19], v[172:175]\n"
    "v_exp_f32 v45, v45\n"
    "v_mfma_f32_16x16x32_bf16 v[176:179], a[112:115], v[16:19], v[176:179]\n"
    "v_cvt_pk_bf16_f32 v47, v46, v47\n"
    "v_cvt_pk_bf16_f32 v46, v44, v45\n"
    "v_mfma_f32_16x16x32_bf16 v[180:183], a[116:119], v[16:19], v[180:183]\n"
    "v_exp_f32 v42, v42\n"
    "v_mfma_f32_16x16x32_bf16 v[184:187], a[120:123], v[16:19], v[184:187]\n"
    "v_exp_f32 v43, v43\n"
    "v_mfma_f32_16x16x32_bf16 v[188:191], a[124:127], v[16:19], v[188:191]\n"
    "v_exp_f32 v40, v40\n"
    "v_mfma_f32_16x16x32_bf16 a[136:139], a[144:147], v[16:19], a[136:139]\n"
    "v_exp_f32 v41, v41\n"
    "s_waitcnt lgkmcnt(0)\n"
    "v_mfma_f32_16x16x32_bf16 a[128:131], a[144:147], v[32:35], a[128:131]\n"
    "v_cvt_pk_bf16_f32 v45, v42, v43\n"
    "v_cvt_pk_bf16_f32 v44, v40, v41\n"
    "v_mfma_f32_16x16x32_bf16 v[96:99], a[64:67], v[32:35], v[96:99]\n"
    "v_exp_f32 v48, v48\n"
    "v_mfma_f32_16x16x32_bf16 v[100:103], a[68:71], v[32:35], v[100:103]\n"
    "v_exp_f32 v49, v49\n"
    "v_mfma_f32_16x16x32_bf16 v[104:107], a[72:75], v[32:35], v[104:107]\n"
    "v_exp_f32 v50, v50\n"
    "v_mfma_f32_16x16x32_bf16 v[108:111], a[76:79], v[32:35], v[108:111]\n"
    "v_exp_f32 v51, v51\n"
    "v_mfma_f32_16x16x32_bf16 v[112:115], a[80:83], v[32:35], v[112:115]\n"
    "v_cvt_pk_bf16_f32 v48, v48, v49\n"
    "v_cvt_pk_bf16_f32 v49, v50, v51\n"
    "v_mfma_f32_16x16x32_bf16 v[116:119], a[84:87], v[32:35], v[116:119]\n"
    "v_exp_f32 v52, v52\n"
    "v_mfma_f32_16x16x32_bf16 v[120:123], a[88:91], v[32:35], v[120:123]\n"
    "v_exp_f32 v53, v53\n"
    "v_mfma_f32_16x16x32_bf16 v[124:127], a[92:95], v[32:35], v[124:127]\n"
    "v_exp_f32 v54, v54\n"
    "v_mfma_f32_16x16x32_bf16 a[132:135], a[144:147], v[44:47], a[132:135]\n"
    "v_exp_f32 v55, v55\n"
    "v_mfma_f32_16x16x32_bf16 v[128:131], a[64:67], v[44:47], v[128:131]\n"
    "v_cvt_pk_bf16_f32 v50, v52, v53\n"
    "v_cvt_pk_bf16_f32 v51, v54, v55\n"
    "v_mfma_f32_16x16x32_bf16 v[132:135], a[68:71], v[44:47], v[132:135]\n"
    "v_accvgpr_read_b32 v88, a128\n"
    "v_cmp_nge_f32 vcc, s61, v88\n"
    "v_mfma_f32_16x16x32_bf16 v[136:139], a[72:75], v[44:47], v[136:139]\n"
    "v_rcp_f32 v64, v88\n"
    "v_mfma_f32_16x16x32_bf16 v[140:143], a[76:79], v[44:47], v[140:143]\n"
    "s_or_b64 s[76:77], s[76:77], vcc\n"
    "v_mov_b32 v65, v64\n"
    "v_mfma_f32_16x16x32_bf16 v[144:147], a[80:83], v[44:47], v[144:147]\n"
    "v_mul_f32 v96, v96, v64\n"
    "v_mul_f32 v97, v97, v64\n"
    "v_mfma_f32_16x16x32_bf16 v[148:151], a[84:87], v[44:47], v[148:151]\n"
    "v_mul_f32 v98, v98, v64\n"
    "v_mul_f32 v99, v99, v64\n"
    "v_mfma_f32_16x16x32_bf16 v[152:155], a[88:91], v[44:47], v[152:155]\n"
    "v_cvt_pk_bf16_f32 v96, v96, v97\n"
    "v_cvt_pk_bf16_f32 v97, v98, v99\n"
    "v_mfma_f32_16x16x32_bf16 v[156:159], a[92:95], v[44:47], v[156:159]\n"
    "ds_write_b64 v251, v[96:97] offset:0\n"
    "v_mul_f32 v100, v100, v64\n"
    "v_mfma_f32_16x16x32_bf16 a[136:139], a[144:147], v[48:51], a[136:139]\n"
    "v_mul_f32 v101, v101, v64\n"
    "v_mul_f32 v102, v102, v64\n"
    "v_mfma_f32_16x16x32_bf16 v[160:163], a[64:67], v[48:51], v[160:163]\n"
    "v_mul_f32 v103, v103, v64\n"
    "v_cvt_pk_bf16_f32 v100, v100, v101\n"
    "v_mfma_f32_16x16x32_bf16 v[164:167], a[68:71], v[48:51], v[164:167]\n"
    "v_cvt_pk_bf16_f32 v101, v102, v103\n"
    "v_mul_f32 v104, v104, v64\n"
    "v_mfma_f32_16x16x32_bf16 v[168:171], a[72:75], v[48:51], v[168:171]\n"
    "ds_write_b64 v251, v[100:101] offset:32\n"
    "v_mul_f32 v105, v105, v64\n"
    "v_mfma_f32_16x16x32_bf16 v[172:175], a[76:79], v[48:51], v[172:175]\n"
    "v_mul_f32 v106, v106, v64\n"
    "v_mul_f32 v107, v107, v64\n"
    "v_mfma_f32_16x16x32_bf16 v[176:179], a[80:83], v[48:51], v[176:179]\n"
    "v_cvt_pk_bf16_f32 v104, v104, v105\n"
    "v_cvt_pk_bf16_f32 v105, v106, v107\n"
    "v_mfma_f32_16x16x32_bf16 v[180:183], a[84:87], v[48:51], v[180:183]\n"
    "ds_write_b64 v251, v[104:105] offset:64\n"
    "v_mul_f32 v108, v108, v64\n"
    "v_mfma_f32_16x16x32_bf16 v[184:187], a[88:91], v[48:51], v[184:187]\n"
    "v_mul_f32 v109, v109, v64\n"
    "v_mul_f32 v110, v110, v64\n"
    "v_mfma_f32_16x16x32_bf16 v[188:191], a[92:95], v[48:51], v[188:191]\n"
    "v_mul_f32 v111, v111, v64\n"
    "v_cvt_pk_bf16_f32 v108, v108, v109\n"
    "v_cvt_pk_bf16_f32 v109, v110, v111\n"
    "v_pk_mul_f32 v[112:113], v[112:113], v[64:65]\n"
    "v_pk_mul_f32 v[114:115], v[114:115], v[64:65]\n"
    "v_cvt_pk_bf16_f32 v112, v112, v113\n"
    "v_cvt_pk_bf16_f32 v113, v114, v115\n"
    "v_pk_mul_f32 v[116:117], v[116:117], v[64:65]\n"
    "v_pk_mul_f32 v[118:119], v[118:119], v[64:65]\n"
    "v_cvt_pk_bf16_f32 v116, v116, v117\n"
    "v_cvt_pk_bf16_f32 v117, v118, v119\n"
    "ds_write_b64 v251, v[108:109] offset:96\n"
    "ds_write_b64 v251, v[112:113] offset:128\n"
    "ds_write_b64 v251, v[116:117] offset:160\n"
    "v_pk_mul_f32 v[120:121], v[120:121], v[64:65]\n"
    "v_pk_mul_f32 v[122:123], v[122:123], v[64:65]\n"
    "v_cvt_pk_bf16_f32 v120, v120, v121\n"
    "v_cvt_pk_bf16_f32 v121, v122, v123\n"
    "v_pk_mul_f32 v[124:125], v[124:125], v[64:65]\n"
    "v_pk_mul_f32 v[126:127], v[126:127], v[64:65]\n"
    "v_cvt_pk_bf16_f32 v124, v124, v125\n"
    "v_cvt_pk_bf16_f32 v125, v126, v127\n"
    "ds_write_b64 v251, v[120:121] offset:192\n"
    "ds_write_b64 v251, v[124:125] offset:224\n"
    "ds_read_b128 v[0:3], v252 offset:0\n"
    "ds_read_b128 v[4:7], v252 offset:1088\n"
    "ds_read_b128 v[8:11], v252 offset:2176\n"
    "ds_read_b128 v[12:15], v252 offset:3264\n"
    "v_accvgpr_read_b32 v89, a132\n"
    "v_accvgpr_read_b32 v66, a152\n"
    "v_accvgpr_read_b32 v67, a153\n"
    "v_add_f32 v89, v89, v66\n"
    "v_add_f32 v89, v89, v67\n"
    "v_cmp_nge_f32 vcc, s61, v89\n"
    "v_rcp_f32 v66, v89\n"
    "s_or_b64 s[76:77], s[76:77], vcc\n"
    "v_mov_b32 v67, v66\n"
    "v_pk_mul_f32 v[128:129], v[128:129], v[66:67]\n"
    "v_pk_mul_f32 v[130:131], v[130:131], v[66:67]\n"
    "v_cvt_pk_bf16_f32 v128, v128, v129\n"
    "v_cvt_pk_bf16_f32 v129, v130, v131\n"
    "v_pk_mul_f32 v[132:133], v[132:133], v[66:67]\n"
    "v_pk_mul_f32 v[134:135], v[134:135], v[66:67]\n"
    "v_cvt_pk_bf16_f32 v132, v132, v133\n"
    "v_cvt_pk_bf16_f32 v133, v134, v135\n"
    "s_mov_b32 s53, 0\n"
    "s_waitcnt lgkmcnt(3)\n"
    "buffer_store_dwordx4 v[0:3], v253, s[48:51], s53 offen nt\n"
    "ds_write_b64 v251, v[128:129] offset:4352\n"
    "ds_write_b64 v251, v[132:133] offset:4384\n"
    "v_pk_mul_f32 v[136:137], v[136:137], v[66:67]\n"
    "v_pk_mul_f32 v[138:139], v[138:139], v[66:67]\n"
    "v_cvt_pk_bf16_f32 v136, v136, v137\n"
    "v_cvt_pk_bf16_f32 v137, v138, v139\n"
    "v_pk_mul_f32 v[140:141], v[140:141], v[66:67]\n"
    "v_pk_mul_f32 v[142:143], v[142:143], v[66:67]\n"
    "v_cvt_pk_bf16_f32 v140, v140, v141\n"
    "v_cvt_pk_bf16_f32 v141, v142, v143\n"
    "s_mov_b32 s53, 16384\n"
    "s_waitcnt lgkmcnt(4)\n"
    "buffer_store_dwordx4 v[4:7], v253, s[48:51], s53 offen nt\n"
    "ds_write_b64 v251, v[136:137] offset:4416\n"
    "ds_write_b64 v251, v[140:141] offset:4448\n"
    "v_pk_mul_f32 v[144:145], v[144:145], v[66:67]\n"
    "v_pk_mul_f32 v[146:147], v[146:147], v[66:67]\n"
    "v_cvt_pk_bf16_f32 v144, v144, v145\n"
    "v_cvt_pk_bf16_f32 v145, v146, v147\n"
    "v_pk_mul_f32 v[148:149], v[148:149], v[66:67]\n"
    "v_pk_mul_f32 v[150:151], v[150:151], v[66:67]\n"
    "v_cvt_pk_bf16_f32 v148, v148, v149\n"
    "v_cvt_pk_bf16_f32 v149, v150, v151\n"
    "s_mov_b32 s53, 32768\n"
    "s_waitcnt lgkmcnt(5)\n"
    "buffer_store_dwordx4 v[8:11], v253, s[48:51], s53 offen nt\n"
    "ds_write_b64 v251, v[144:145] offset:4480\n"
    "ds_write_b64 v251, v[148:149] offset:4512\n"
    "v_pk_mul_f32 v[152:153], v[152:153], v[66:67]\n"
    "v_pk_mul_f32 v[154:155], v[154:155], v[66:67]\n"
    "v_cvt_pk_bf16_f32 v152, v152, v153\n"
    "v_cvt_pk_bf16_f32 v153, v154, v155\n"
    "v_pk_mul_f32 v[156:157], v[156:157], v[66:67]\n"
    "v_pk_mul_f32 v[158:159], v[158:159], v[66:67]\n"
    "v_cvt_pk_bf16_f32 v156, v156, v157\n"
    "v_cvt_pk_bf16_f32 v157, v158, v159\n"
    "s_mov_b32 s53, 49152\n"
    "s_waitcnt lgkmcnt(6)\n"
    "buffer_store_dwordx4 v[12:15], v253, s[48:51], s53 offen nt\n"
    "ds_write_b64 v251, v[152:153] offset:4544\n"
    "ds_write_b64 v251, v[156:157] offset:4576\n"
    "ds_read_b128 v[16:19], v252 offset:4352\n"
    "ds_read_b128 v[20:23], v252 offset:5440\n"
    "ds_read_b128 v[24:27], v252 offset:6528\n"
    "ds_read_b128 v[28:31], v252 offset:7616\n"
    "v_accvgpr_read_b32 v90, a136\n"
    "v_accvgpr_read_b32 v68, a154\n"
    "v_accvgpr_read_b32 v69, a155\n"
    "v_add_f32 v90, v90, v68\n"
    "v_add_f32 v90, v90, v69\n"
    "v_cmp_nge_f32 vcc, s61, v90\n"
    "v_rcp_f32 v68, v90\n"
    "s_or_b64 s[76:77], s[76:77], vcc\n"
    "v_mov_b32 v69, v68\n"
    "v_pk_mul_f32 v[160:161], v[160:161], v[68:69]\n"
    "v_pk_mul_f32 v[162:163], v[162:163], v[68:69]\n"
    "v_cvt_pk_bf16_f32 v160, v160, v161\n"
    "v_cvt_pk_bf16_f32 v161, v162, v163\n"
    "v_pk_mul_f32 v[164:165], v[164:165], v[68:69]\n"
    "v_pk_mul_f32 v[166:167], v[166:167], v[68:69]\n"
    "v_cvt_pk_bf16_f32 v164, v164, v165\n"
    "v_cvt_pk_bf16_f32 v165, v166, v167\n"
    "s_mov_b32 s53, 65536\n"
    "s_waitcnt lgkmcnt(3)\n"
    "buffer_store_dwordx4 v[16:19], v253, s[48:51], s53 offen nt\n"
    "ds_write_b64 v251, v[160:161] offset:8704\n"
    "ds_write_b64 v251, v[164:165] offset:8736\n"
    "v_pk_mul_f32 v[168:169], v[168:169], v[68:69]\n"
    "v_pk_mul_f32 v[170:171], v[170:171], v[68:69]\n"
    "v_cvt_pk_bf16_f32 v168, v168, v169\n"
    "v_cvt_pk_bf16_f32 v169, v170, v171\n"
    "v_pk_mul_f32 v[172:173], v[172:173], v[68:69]\n"
    "v_pk_mul_f32 v[174:175], v[174:175], v[68:69]\n"
    "v_cvt_pk_bf16_f32 v172, v172, v173\n"
    "v_cvt_pk_bf16_f32 v173, v174, v175\n"
    "s_mov_b32 s53, 81920\n"
    "s_waitcnt lgkmcnt(4)\n"
    "buffer_store_dwordx4 v[20:23], v253, s[48:51], s53 offen nt\n"
    "ds_write_b64 v251, v[168:169] offset:8768\n"
    "ds_write_b64 v251, v[172:173] offset:8800\n"
    "v_pk_mul_f32 v[176:177], v[176:177], v[68:69]\n"
    "v_pk_mul_f32 v[178:179], v[178:179], v[68:69]\n"
    "v_cvt_pk_bf16_f32 v176, v176, v177\n"
    "v_cvt_pk_bf16_f32 v177, v178, v179\n"
    "v_pk_mul_f32 v[180:181], v[180:181], v[68:69]\n"
    "v_pk_mul_f32 v[182:183], v[182:183], v[68:69]\n"
    "v_cvt_pk_bf16_f32 v180, v180, v181\n"
    "v_cvt_pk_bf16_f32 v181, v182, v183\n"
    "s_mov_b32 s53, 98304\n"
    "s_waitcnt lgkmcnt(5)\n"
    "buffer_store_dwordx4 v[24:27], v253, s[48:51], s53 offen nt\n"
    "ds_write_b64 v251, v[176:177] offset:8832\n"
    "ds_write_b64 v251, v[180:181] offset:8864\n"
    "v_pk_mul_f32 v[184:185], v[184:185], v[68:69]\n"
    "v_pk_mul_f32 v[186:187], v[186:187], v[68:69]\n"
    "v_cvt_pk_bf16_f32 v184, v184, v185\n"
    "v_cvt_pk_bf16_f32 v185, v186, v187\n"
    "v_pk_mul_f32 v[188:189], v[188:189], v[68:69]\n"
    "v_pk_mul_f32 v[190:191], v[190:191], v[68:69]\n"
    "v_cvt_pk_bf16_f32 v188, v188, v189\n"
    "v_cvt_pk_bf16_f32 v189, v190, v191\n"
    "s_mov_b32 s53, 114688\n"
    "s_waitcnt lgkmcnt(6)\n"
    "buffer_store_dwordx4 v[28:31], v253, s[48:51], s53 offen nt\n"
    "ds_write_b64 v251, v[184:185] offset:8896\n"
    "ds_write_b64 v251, v[188:189] offset:8928\n"
    "ds_read_b128 v[0:3], v252 offset:8704\n"
    "ds_read_b128 v[4:7], v252 offset:9792\n"
    "ds_read_b128 v[8:11], v252 offset:10880\n"
    "ds_read_b128 v[12:15], v252 offset:11968\n"
    "s_mov_b32 s53, 131072\n"
    "s_waitcnt lgkmcnt(3)\n"
    "buffer_store_dwordx4 v[0:3], v253, s[48:51], s53 offen nt\n"
    "s_mov_b32 s53, 147456\n"
    "s_waitcnt lgkmcnt(2)\n"
    "buffer_store_dwordx4 v[4:7], v253, s[48:51], s53 offen nt\n"
    "s_mov_b32 s53, 163840\n"
    "s_waitcnt lgkmcnt(1)\n"
    "buffer_store_dwordx4 v[8:11], v253, s[48:51], s53 offen nt\n"
    "s_mov_b32 s53, 180224\n"
    "s_waitcnt lgkmcnt(0)\n"
    "buffer_store_dwordx4 v[12:15], v253, s[48:51], s53 offen nt\n"
    "s_or_b32 %0, s76, s77\n"
    "L3_end_%=:\n"
    "Lk_end3:\n"
    "s_nop 1\n"
    : "=&s"(flag) : "s"(kb), "s"(nrk), "s"(vb), "s"(qp), "s"(nrq), "s"(op), "s"(nh), "s"(nv), "s"(w), "s"(ldsb), "s"(nv0), "a"(k0), "a"(k1)
    : "v0", "v1", "v2", "v3", "v4", "v5", "v6", "v7", "v8", "v9", "v10", "v11", "v12", "v13", "v14", "v15", "v16", "v17", "v18", "v19", "v20", "v21", "v22", "v23", "v24", "v25", "v26", "v27", "v28", "v29", "v30", "v31", "v32", "v33", "v34", "v35", "v36", "v37", "v38", "v39", "v40", "v41", "v42", "v43", "v44", "v45", "v46", "v47", "v48", "v49", "v50", "v51", "v52", "v53", "v54", "v55", "v56", "v57", "v58", "v59", "v60", "v61", "v62", "v63", "v64", "v65", "v66", "v67", "v68", "v69", "v70", "v71", "v72", "v73", "v74", "v75", "v76", "v77", "v78", "v79", "v80", "v81", "v82", "v83", "v84", "v85", "v86", "v87", "v88", "v89", "v90", "v91", "v92", "v93", "v94", "v95", "v96", "v97", "v98", "v99", "v100", "v101", "v102", "v103", "v104", "v105", "v106", "v107", "v108", "v109", "v110", "v111", "v112", "v113", "v114", "v115", "v116", "v117", "v118", "v119", "v120", "v121", "v122", "v123", "v124", "v125", "v126", "v127", "v128", "v129", "v130", "v131", "v132", "v133", "v134", "v135", "v136", "v137", "v138", "v139", "v140", "v141", "v142", "v143", "v144", "v145", "v146", "v147", "v148", "v149", "v150", "v151", "v152", "v153", "v154", "v155", "v156", "v157", "v158", "v159", "v160", "v161", "v162", "v163", "v164", "v165", "v166", "v167", "v168", "v169", "v170", "v171", "v172", "v173", "v174", "v175", "v176", "v177", "v178", "v179", "v180", "v181", "v182", "v183", "v184", "v185", "v186", "v187", "v188", "v189", "v190", "v191", "v192", "v193", "v194", "v195", "v196", "v197", "v198", "v199", "v200", "v201", "v202", "v203", "v204", "v205", "v206", "v207", "v208", "v209", "v210", "v211", "v212", "v213", "v214", "v215", "v216", "v217", "v218", "v219", "v220", "v221", "v222", "v223", "v224", "v225", "v226", "v227", "v228", "v229", "v230", "v231", "v232", "v233", "v234", "v235", "v236", "v237", "v238", "v239", "v240", "v241", "v242", "v243", "v244", "v245", "v246", "v247", "v248", "v249", "v250", "v251", "v252", "v253", "a0", "a1", "a2", "a3", "a4", "a5", "a6", "a7", "a8", "a9", "a10", "a11", "a12", "a13", "a14", "a15", "a16", "a17", "a18", "a19", "a20", "a21", "a22", "a23", "a24", "a25", "a26", "a27", "a28", "a29", "a30", "a31", "a32", "a33", "a34", "a35", "a36", "a37", "a38", "a39", "a40", "a41", "a42", "a43", "a44", "a45", "a46", "a47", "a48", "a49", "a50", "a51", "a52", "a53", "a54", "a55", "a56", "a57", "a58", "a59", "a60", "a61", "a62", "a63", "a64", "a65", "a66", "a67", "a68", "a69", "a70", "a71", "a72", "a73", "a74", "a75", "a76", "a77", "a78", "a79", "a80", "a81", "a82", "a83", "a84", "a85", "a86", "a87", "a88", "a89", "a90", "a91", "a92", "a93", "a94", "a95", "a96", "a97", "a98", "a99", "a100", "a101", "a102", "a103", "a104", "a105", "a106", "a107", "a108", "a109", "a110", "a111", "a112", "a113", "a114", "a115", "a116", "a117", "a118", "a119", "a120", "a121", "a122", "a123", "a124", "a125", "a126", "a127", "a128", "a129", "a130", "a131", "a132", "a133", "a134", "a135", "a136", "a137", "a138", "a139", "a140", "a141", "a142", "a143", "a144", "a145", "a146", "a147", "a148", "a152", "a153", "a154", "a155", "a156", "a157", "a158", "a159", "a160", "a161", "a162", "a163", "a164", "a165", "a166", "a167", "a168", "a169", "a170", "a171", "a172", "a173", "a174", "a175", "a176", "a177", "a178", "a179", "a180", "a181", "a182", "a183", "a184", "a185", "a186", "a187", "a188", "a189", "a190", "a191", "a192", "a193", "a194", "a195", "s36", "s37", "s38", "s39", "s40", "s41", "s42", "s43", "s44", "s45", "s46", "s47", "s48", "s49", "s50", "s51", "s52", "s53", "s54", "s55", "s56", "s57", "s58", "s59", "s60", "s61", "s62", "s63", "s64", "s65", "s66", "s67", "s68", "s69", "s70", "s71", "s72", "s73", "s74", "s75", "s76", "s77", "vcc", "scc", "memory");
  return flag;
}

__device__ __forceinline__ unsigned seg2(uint64_t kb, unsigned nrk, uint64_t vb, uint64_t qp, unsigned nrq, uint64_t op, unsigned nh, unsigned nv, unsigned w, unsigned ldsb, unsigned nv0, unsigned k0, unsigned k1) {
  unsigned flag;
  asm volatile(
    "s_mov_b64 s[36:37], %1\n"
    "s_mov_b32 s38, %2\n"
    "s_mov_b32 s39, 0x20000\n"
    "s_mov_b64 s[44:45], %4\n"
    "s_mov_b32 s46, %5\n"
    "s_mov_b32 s47, 0x20000\n"
    "s_mov_b32 s60, %9\n"
    "s_mov_b32 s67, %10\n"
    "v_mbcnt_lo_u32_b32 v236, -1, 0\n"
    "v_mbcnt_hi_u32_b32 v236, -1, v236\n"
    "v_lshrrev_b32 v237, 4, v236\n"
    "v_bfe_u32 v238, v236, 2, 3\n"
    "v_lshlrev_b32 v238, 12, v238\n"
    "v_lshrrev_b32 v239, 5, v236\n"
    "v_lshl_add_u32 v238, v239, 6, v238\n"
    "v_and_b32 v240, 3, v236\n"
    "v_lshl_add_u32 v241, v240, 4, v238\n"
    "s_and_b32 s61, s60, 1\n"
    "s_mul_i32 s61, s61, 48\n"
    "v_xor_b32 v243, s61, v241\n"
    "s_lshl_b32 s61, s60, 15\n"
    "s_add_u32 s61, s61, 4096\n"
    "v_add_u32 v243, s61, v243\n"
    "v_add_u32 v240, -896, v243\n"
    "s_lshl_b32 s61, s60, 11\n"
    "s_add_u32 s61, s61, s67\n"
    "s_add_u32 m0, s61, 73728\n"
    "s_nop 0\n"
    "buffer_load_dwordx4 v243, s[36:39], 0 offen nt lds\n"
    "buffer_load_dwordx4 v240, s[36:39], 0 offen offset:1024 nt lds\n"
    "v_xor_b32 v242, 48, v241\n"
    "s_lshl_b32 s61, s60, 16\n"
    "s_add_u32 s61, s61, 4096\n"
    "v_add_u32 v228, s61, v241\n"
    "v_add_u32 v229, -896, v228\n"
    "v_add_u32 v230, s61, v242\n"
    "v_add_u32 v231, 29824, v230\n"
    "v_add_u32 v230, 30720, v230\n"
    "s_lshl_b32 s54, s60, 12\n"
    "s_add_u32 s54, s54, s67\n"
    "s_mov_b32 s52, 0x20000\n"
    "s_sub_u32 s44, s44, 4096\n"
    "s_subb_u32 s45, s45, 0\n"
    "s_add_u32 s46, s46, 4096\n"
    "s_lshl_b32 s64, s60, 14\n"
    "s_add_u32 s64, s64, 98304\n"
    "s_add_u32 s76, s64, s67\n"
    "s_mul_i32 s77, s60, 65536\n"
    "s_add_u32 m0, s76, 0\n"
    "s_nop 0\n"
    "buffer_load_dwordx4 v228, s[44:47], s77 offen offset:0 nt lds\n"
    "buffer_load_dwordx4 v230, s[44:47], s77 offen offset:2048 nt lds\n"
    "buffer_load_dwordx4 v229, s[44:47], s77 offen offset:1024 nt lds\n"
    "buffer_load_dwordx4 v231, s[44:47], s77 offen offset:3072 nt lds\n"
    "v_accvgpr_write_b32 a96, 0\n"
    "v_accvgpr_write_b32 a97, 0\n"
    "v_accvgpr_write_b32 a98, 0\n"
    "v_accvgpr_write_b32 a99, 0\n"
    "s_add_u32 m0, s54, 0\n"
    "s_nop 0\n"
    "buffer_load_dwordx4 v228, s[36:39], s52 offen offset:0 nt lds\n"
    "s_mov_b64 s[40:41], %3\n"
    "s_mov_b32 s42, %2\n"
    "v_mfma_f32_32x32x16_bf16 v[96:111], a[96:99], a[96:99], 0\n"
    "s_mov_b32 s43, 0x20000\n"
    "v_bfe_u32 v238, v236, 1, 3\n"
    "v_lshlrev_b32 v238, 12, v238\n"
    "v_lshl_add_u32 v238, v237, 5, v238\n"
    "v_and_b32 v239, 1, v236\n"
    "v_lshl_add_u32 v238, v239, 4, v238\n"
    "buffer_load_dwordx4 v229, s[36:39], s52 offen offset:1024 nt lds\n"
    "v_add_u32 v232, s61, v238\n"
    "v_add_u32 v233, -896, v232\n"
    "v_add_u32 v234, 30720, v232\n"
    "v_add_u32 v235, 29824, v232\n"
    "s_add_u32 s55, s54, 16384\n"
    "s_mov_b32 s53, 0\n"
    "s_mov_b64 s[48:49], %6\n"
    "s_mov_b32 s50, %5\n"
    "v_mfma_f32_32x32x16_bf16 v[112:127], a[96:99], a[96:99], 0\n"
    "buffer_load_dwordx4 v230, s[36:39], s52 offen offset:2048 nt lds\n"
    "s_mov_b32 s51, 0x20000\n"
    "s_mov_b32 s66, %7\n"
    "s_mov_b32 s65, %8\n"
    "s_mov_b32 s62, 0x3e0293ee\n"
    "s_mov_b32 s63, 0x3e0293ee\n"
    "v_bfe_u32 v238, v236, 3, 1\n"
    "v_mul_u32_u24 v239, 3, v238\n"
    "v_xor_b32 v239, v239, v237\n"
    "buffer_load_dwordx4 v231, s[36:39], s52 offen offset:3072 nt lds\n"
    "v_lshlrev_b32 v238, 11, v238\n"
    "v_lshl_add_u32 v238, v239, 4, v238\n"
    "v_and_b32 v239, 7, v236\n"
    "v_lshl_add_u32 v238, v239, 6, v238\n"
    "v_add_u32 v226, s67, v238\n"
    "v_lshrrev_b32 v238, 5, v236\n"
    "v_mfma_f32_32x32x16_bf16 v[128:143], a[96:99], a[96:99], 0\n"
    "v_lshlrev_b32 v238, 11, v238\n"
    "v_and_b32 v239, 31, v236\n"
    "s_add_u32 m0, s76, 4096\n"
    "s_add_u32 s77, s77, 65536\n"
    "buffer_load_dwordx4 v228, s[44:47], s77 offen offset:0 nt lds\n"
    "v_lshl_add_u32 v238, v239, 3, v238\n"
    "v_add_u32 v238, 16384, v238\n"
    "v_add_u32 v227, s67, v238\n"
    "v_mov_b32 v224, v226\n"
    "v_mov_b32 v225, v227\n"
    "s_mov_b32 s58, 0\n"
    "s_mov_b32 s59, 65536\n"
    "v_lshlrev_b32 v238, 2, v237\n"
    "buffer_load_dwordx4 v230, s[44:47], s77 offen offset:2048 nt lds\n"
    "v_sub_u32 v248, s65, v238\n"
    "v_sub_u32 v250, %11, v238\n"
    "v_mov_b32 v249, 0xff800000\n"
    "v_and_b32 v238, 3, v236\n"
    "v_mfma_f32_32x32x16_bf16 v[144:159], a[96:99], a[96:99], 0\n"
    "v_cmp_eq_u32 vcc, 0, v238\n"
    "v_mov_b32 v239, 0x3f803f80\n"
    "s_nop 0\n"
    "v_cndmask_b32 v238, 0, v239, vcc\n"
    "buffer_load_dwordx4 v229, s[44:47], s77 offen offset:1024 nt lds\n"
    "v_accvgpr_write_b32 a144, v238\n"
    "v_accvgpr_write_b32 a145, v238\n"
    "v_accvgpr_write_b32 a146, v238\n"
    "v_accvgpr_write_b32 a147, v238\n"
    "s_cmp_lt_u32 s65, 32\n"
    "s_cselect_b32 s57, 1, 0\n"
    "s_cmp_ge_u32 s66, 3\n"
    "s_cselect_b32 s57, s57, 0\n"
    "buffer_load_dwordx4 v231, s[44:47], s77 offen offset:3072 nt lds\n"
    "s_sub_u32 s56, s66, s57\n"
    "s_sub_u32 s56, s56, 2\n"
    "v_mfma_f32_32x32x16_bf16 a[128:143], a[96:99], a[96:99], 0\n"
    "s_add_u32 s61, s64, s67\n"
    "v_and_b32 v238, 15, v236\n"
    "v_mul_u32_u24 v239, 272, v238\n"
    "v_lshl_add_u32 v239, v237, 3, v239\n"
    "v_add_u32 v251, s61, v239\n"
    "v_mul_u32_u24 v239, 272, v237\n"
    "v_lshl_add_u32 v239, v238, 4, v239\n"
    "v_add_u32 v252, s61, v239\n"
    "s_mul_i32 s61, s60, 32\n"
    "v_add_u32 v239, s61, v237\n"
    "v_lshlrev_b32 v239, 12, v239\n"
    "v_lshl_add_u32 v253, v238, 4, v239\n"
    "v_add_u32 v243, s64, v226\n"
    "v_add_u32 v242, 73728, v226\n"
    "s_waitcnt vmcnt(10)\n"
    "ds_read_b128 v[160:163], v243 offset:0\n"
    "ds_read_b128 v[164:167], v243 offset:512\n"
    "s_waitcnt vmcnt(8)\n"
    "ds_read_b128 v[168:171], v243 offset:1024\n"
    "ds_read_b128 v[172:175], v243 offset:1536\n"
    "s_waitcnt lgkmcnt(2)\n"
    "v_lshlrev_b32 v88, 16, v160\n"
    "v_and_b32 v89, 0xffff0000, v160\n"
    "v_pk_mul_f32 v[88:89], v[88:89], s[62:63]\n"
    "v_cvt_pk_bf16_f32 v160, v88, v89\n"
    "v_lshlrev_b32 v90, 16, v161\n"
    "v_and_b32 v91, 0xffff0000, v161\n"
    "v_pk_mul_f32 v[90:91], v[90:91], s[62:63]\n"
    "v_cvt_pk_bf16_f32 v161, v90, v91\n"
    "s_add_u32 m0, s55, 0\n"
    "s_add_u32 s52, s52, 0x40000\n"
    "buffer_load_dwordx4 v232, s[40:43], s53 offen offset:0 nt lds\n"
    "v_lshlrev_b32 v88, 16, v162\n"
    "v_and_b32 v89, 0xffff0000, v162\n"
    "v_pk_mul_f32 v[88:89], v[88:89], s[62:63]\n"
    "v_cvt_pk_bf16_f32 v162, v88, v89\n"
    "v_lshlrev_b32 v90, 16, v163\n"
    "v_and_b32 v91, 0xffff0000, v163\n"
    "v_pk_mul_f32 v[90:91], v[90:91], s[62:63]\n"
    "v_cvt_pk_bf16_f32 v163, v90, v91\n"
    "buffer_load_dwordx4 v233, s[40:43], s53 offen offset:1024 nt lds\n"
    "v_lshlrev_b32 v88, 16, v164\n"
    "v_and_b32 v89, 0xffff0000, v164\n"
    "v_pk_mul_f32 v[88:89], v[88:89], s[62:63]\n"
    "v_cvt_pk_bf16_f32 v164, v88, v89\n"
    "v_lshlrev_b32 v90, 16, v165\n"
    "v_and_b32 v91, 0xffff0000, v165\n"
    "v_pk_mul_f32 v[90:91], v[90:91], s[62:63]\n"
    "v_cvt_pk_bf16_f32 v165, v90, v91\n"
    "buffer_load_dwordx4 v234, s[40:43], s53 offen offset:2048 nt lds\n"
    "v_lshlrev_b32 v88, 16, v166\n"
    "v_and_b32 v89, 0xffff0000, v166\n"
    "v_pk_mul_f32 v[88:89], v[88:89], s[62:63]\n"
    "v_cvt_pk_bf16_f32 v166, v88, v89\n"
    "v_lshlrev_b32 v90, 16, v167\n"
    "v_and_b32 v91, 0xffff0000, v167\n"
    "v_pk_mul_f32 v[90:91], v[90:91], s[62:63]\n"
    "v_cvt_pk_bf16_f32 v167, v90, v91\n"
    "buffer_load_dwordx4 v235, s[40:43], s53 offen offset:3072 nt lds\n"
    "s_add_u32 s53, s53, 0x40000\n"
    "s_waitcnt vmcnt(8)\n"
    "s_barrier\n"
    "ds_read_b128 a[64:67], v242 offset:0\n"
    "ds_read_b128 a[68:71], v242 offset:512\n"
    "ds_read_b128 a[72:75], v242 offset:1024\n"
    "ds_read_b128 a[76:79], v242 offset:1536\n"
    "ds_read_b128 a[80:83], v242 offset:4096\n"
    "ds_read_b128 a[84:87], v242 offset:4608\n"
    "ds_read_b128 a[88:91], v242 offset:5120\n"
    "ds_read_b128 a[92:95], v242 offset:5632\n"
    "ds_read_b128 a[160:163], v226 offset:0\n"
    "ds_read_b128 a[164:167], v226 offset:512\n"
    "ds_read_b128 a[168:171], v226 offset:1024\n"
    "ds_read_b128 a[172:175], v226 offset:1536\n"
    "ds_read_b128 a[176:179], v226 offset:4096\n"
    "ds_read_b128 a[180:183], v226 offset:4608\n"
    "ds_read_b128 a[184:187], v226 offset:5120\n"
    "ds_read_b128 a[188:191], v226 offset:5632\n"
    "s_waitcnt lgkmcnt(15)\n"
    "v_lshlrev_b32 v88, 16, v168\n"
    "v_and_b32 v89, 0xffff0000, v168\n"
    "v_pk_mul_f32 v[88:89], v[88:89], s[62:63]\n"
    "v_cvt_pk_bf16_f32 v168, v88, v89\n"
    "v_lshlrev_b32 v90, 16, v169\n"
    "v_and_b32 v91, 0xffff0000, v169\n"
    "v_pk_mul_f32 v[90:91], v[90:91], s[62:63]\n"
    "v_cvt_pk_bf16_f32 v169, v90, v91\n"
    "s_add_u32 m0, s54, 32768\n"
    "s_nop 0\n"
    "buffer_load_dwordx4 v228, s[36:39], s52 offen offset:0 nt lds\n"
    "v_lshlrev_b32 v88, 16, v170\n"
    "v_and_b32 v89, 0xffff0000, v170\n"
    "v_pk_mul_f32 v[88:89], v[88:89], s[62:63]\n"
    "v_cvt_pk_bf16_f32 v170, v88, v89\n"
    "v_lshlrev_b32 v90, 16, v171\n"
    "v_and_b32 v91, 0xffff0000, v171\n"
    "v_pk_mul_f32 v[90:91], v[90:91], s[62:63]\n"
    "v_cvt_pk_bf16_f32 v171, v90, v91\n"
    "buffer_load_dwordx4 v229, s[36:39], s52 offen offset:1024 nt lds\n"
    "v_lshlrev_b32 v88, 16, v172\n"
    "v_and_b32 v89, 0xffff0000, v172\n"
    "v_pk_mul_f32 v[88:89], v[88:89], s[62:63]\n"
    "v_cvt_pk_bf16_f32 v172, v88, v89\n"
    "v_lshlrev_b32 v90, 16, v173\n"
    "v_and_b32 v91, 0xffff0000, v173\n"
    "v_pk_mul_f32 v[90:91], v[90:91], s[62:63]\n"
    "v_cvt_pk_bf16_f32 v173, v90, v91\n"
    "buffer_load_dwordx4 v230, s[36:39], s52 offen offset:2048 nt lds\n"
    "v_lshlrev_b32 v88, 16, v174\n"
    "v_and_b32 v89, 0xffff0000, v174\n"
    "v_pk_mul_f32 v[88:89], v[88:89], s[62:63]\n"
    "v_cvt_pk_bf16_f32 v174, v88, v89\n"
    "v_lshlrev_b32 v90, 16, v175\n"
    "v_and_b32 v91, 0xffff0000, v175\n"
    "v_pk_mul_f32 v[90:91], v[90:91], s[62:63]\n"
    "v_cvt_pk_bf16_f32 v175, v90, v91\n"
    "buffer_load_dwordx4 v231, s[36:39], s52 offen offset:3072 nt lds\n"
    "s_waitcnt lgkmcnt(8)\n"
    "v_mfma_f32_16x16x32_bf16 v[0:3], a[64:67], v[160:163], 0\n"
    "v_mfma_f32_16x16x32_bf16 v[0:3], a[68:71], v[164:167], v[0:3]\n"
    "v_mfma_f32_16x16x32_bf16 v[0:3], a[72:75], v[168:171], v[0:3]\n"
    "v_mfma_f32_16x16x32_bf16 v[0:3], a[76:79], v[172:175], v[0:3]\n"
    "v_mfma_f32_16x16x32_bf16 v[4:7], a[80:83], v[160:163], 0\n"
    "v_mfma_f32_16x16x32_bf16 v[4:7], a[84:87], v[164:167], v[4:7]\n"
    "v_mfma_f32_16x16x32_bf16 v[4:7], a[88:91], v[168:171], v[4:7]\n"
    "v_mfma_f32_16x16x32_bf16 v[4:7], a[92:95], v[172:175], v[4:7]\n"
    "s_cmp_lt_u32 %11, 32\n"
    "s_cbranch_scc1 L2_m0_0x_%=\n"
    "L2_m0_0_%=:\n"
    "s_waitcnt lgkmcnt(0)\n"
    "s_nop 4\n"
    "v_max3_f32 v244, v0, v1, v2\n"
    "v_max3_f32 v238, v3, v4, v5\n"
    "v_max3_f32 v244, v244, v6, v7\n"
    "v_max_f32 v244, v244, v238\n"
    "v_mov_b32 v238, v244\n"
    "s_nop 1\n"
    "v_permlane16_swap_b32 v244, v238\n"
    "s_nop 1\n"
    "v_max_f32 v244, v244, v238\n"
    "v_mov_b32 v238, v244\n"
    "s_nop 1\n"
    "v_permlane32_swap_b32 v244, v238\n"
    "s_nop 1\n"
    "v_max_f32 v244, v244, v238\n"
    "v_sub_f32 v64, 0, v244\n"
    "v_sub_f32 v65, 0, v244\n"
    "v_sub_f32 v66, 0, v244\n"
    "v_sub_f32 v67, 0, v244\n"
    "v_sub_f32 v68, 0, v244\n"
    "v_sub_f32 v69, 0, v244\n"
    "v_sub_f32 v70, 0, v244\n"
    "v_sub_f32 v71, 0, v244\n"
    "s_cmp_eq_u32 s66, 2\n"
    "s_cbranch_scc1 L2_m1_0x_%=\n"
    "L2_m1_0_%=:\n"
    "s_nop 1\n"
    "v_mfma_f32_16x16x32_bf16 v[32:35], a[160:163], v[160:163], v[64:67]\n"
    "v_sub_f32 v0, v0, v244\n"
    "v_sub_f32 v1, v1, v244\n"
    "v_mfma_f32_16x16x32_bf16 v[32:35], a[164:167], v[164:167], v[32:35]\n"
    "v_sub_f32 v2, v2, v244\n"
    "v_sub_f32 v3, v3, v244\n"
    "v_mfma_f32_16x16x32_bf16 v[32:35], a[168:171], v[168:171], v[32:35]\n"
    "v_exp_f32 v0, v0\n"
    "v_mfma_f32_16x16x32_bf16 v[32:35], a[172:175], v[172:175], v[32:35]\n"
    "v_exp_f32 v1, v1\n"
    "v_mfma_f32_16x16x32_bf16 v[36:39], a[176:179], v[160:163], v[68:71]\n"
    "v_exp_f32 v2, v2\n"
    "v_mfma_f32_16x16x32_bf16 v[36:39], a[180:183], v[164:167], v[36:39]\n"
    "v_exp_f32 v3, v3\n"
    "v_mfma_f32_16x16x32_bf16 v[36:39], a[184:187], v[168:171], v[36:39]\n"
    "v_cvt_pk_bf16_f32 v0, v0, v1\n"
    "v_cvt_pk_bf16_f32 v1, v2, v3\n"
    "v_mfma_f32_16x16x32_bf16 v[36:39], a[188:191], v[172:175], v[36:39]\n"
    "v_sub_f32 v4, v4, v244\n"
    "v_sub_f32 v5, v5, v244\n"
    "s_waitcnt vmcnt(10)\n"
    "ds_read_b128 v[176:179], v243 offset:4096\n"
    "ds_read_b128 v[180:183], v243 offset:4608\n"
    "s_waitcnt vmcnt(8)\n"
    "ds_read_b128 v[184:187], v243 offset:5120\n"
    "ds_read_b128 v[188:191], v243 offset:5632\n"
    "s_waitcnt lgkmcnt(2)\n"
    "v_lshlrev_b32 v88, 16, v176\n"
    "v_and_b32 v89, 0xffff0000, v176\n"
    "v_pk_mul_f32 v[88:89], v[88:89], s[62:63]\n"
    "v_cvt_pk_bf16_f32 v176, v88, v89\n"
    "v_lshlrev_b32 v90, 16, v177\n"
    "v_and_b32 v91, 0xffff0000, v177\n"
    "v_pk_mul_f32 v[90:91], v[90:91], s[62:63]\n"
    "v_cvt_pk_bf16_f32 v177, v90, v91\n"
    "s_add_u32 m0, s55, 32768\n"
    "s_add_u32 s52, s52, 0x40000\n"
    "buffer_load_dwordx4 v232, s[40:43], s53 offen offset:0 nt lds\n"
    "v_lshlrev_b32 v88, 16, v178\n"
    "v_and_b32 v89, 0xffff0000, v178\n"
    "v_pk_mul_f32 v[88:89], v[88:89], s[62:63]\n"
    "v_cvt_pk_bf16_f32 v178, v88, v89\n"
    "v_lshlrev_b32 v90, 16, v179\n"
    "v_and_b32 v91, 0xffff0000, v179\n"
    "v_pk_mul_f32 v[90:91], v[90:91], s[62:63]\n"
    "v_cvt_pk_bf16_f32 v179, v90, v91\n"
    "buffer_load_dwordx4 v233, s[40:43], s53 offen offset:1024 nt lds\n"
    "v_lshlrev_b32 v88, 16, v180\n"
    "v_and_b32 v89, 0xffff0000, v180\n"
    "v_pk_mul_f32 v[88:89], v[88:89], s[62:63]\n"
    "v_cvt_pk_bf16_f32 v180, v88, v89\n"
    "v_lshlrev_b32 v90, 16, v181\n"
    "v_and_b32 v91, 0xffff0000, v181\n"
    "v_pk_mul_f32 v[90:91], v[90:91], s[62:63]\n"
    "v_cvt_pk_bf16_f32 v181, v90, v91\n"
    "buffer_load_dwordx4 v234, s[40:43], s53 offen offset:2048 nt lds\n"
    "v_lshlrev_b32 v88, 16, v182\n"
    "v_and_b32 v89, 0xffff0000, v182\n"
    "v_pk_mul_f32 v[88:89], v[88:89], s[62:63]\n"
    "v_cvt_pk_bf16_f32 v182, v88, v89\n"
    "v_lshlrev_b32 v90, 16, v183\n"
    "v_and_b32 v91, 0xffff0000, v183\n"
    "v_pk_mul_f32 v[90:91], v[90:91], s[62:63]\n"
    "v_cvt_pk_bf16_f32 v183, v90, v91\n"
    "buffer_load_dwordx4 v235, s[40:43], s53 offen offset:3072 nt lds\n"
    "s_add_u32 s53, s53, 0x40000\n"
    "s_waitcnt lgkmcnt(0)\n"
    "v_lshlrev_b32 v88, 16, v184\n"
    "v_and_b32 v89, 0xffff0000, v184\n"
    "v_pk_mul_f32 v[88:89], v[88:89], s[62:63]\n"
    "v_cvt_pk_bf16_f32 v184, v88, v89\n"
    "v_lshlrev_b32 v90, 16, v185\n"
    "v_and_b32 v91, 0xffff0000, v185\n"
    "v_pk_mul_f32 v[90:91], v[90:91], s[62:63]\n"
    "v_cvt_pk_bf16_f32 v185, v90, v91\n"
    "v_lshlrev_b32 v88, 16, v186\n"
    "v_and_b32 v89, 0xffff0000, v186\n"
    "v_pk_mul_f32 v[88:89], v[88:89], s[62:63]\n"
    "v_cvt_pk_bf16_f32 v186, v88, v89\n"
    "v_lshlrev_b32 v90, 16, v187\n"
    "v_and_b32 v91, 0xffff0000, v187\n"
    "v_pk_mul_f32 v[90:91], v[90:91], s[62:63]\n"
    "v_cvt_pk_bf16_f32 v187, v90, v91\n"
    "v_lshlrev_b32 v88, 16, v188\n"
    "v_and_b32 v89, 0xffff0000, v188\n"
    "v_pk_mul_f32 v[88:89], v[88:89], s[62:63]\n"
    "v_cvt_pk_bf16_f32 v188, v88, v89\n"
    "v_lshlrev_b32 v90, 16, v189\n"
    "v_and_b32 v91, 0xffff0000, v189\n"
    "v_pk_mul_f32 v[90:91], v[90:91], s[62:63]\n"
    "v_cvt_pk_bf16_f32 v189, v90, v91\n"
    "v_lshlrev_b32 v88, 16, v190\n"
    "v_and_b32 v89, 0xffff0000, v190\n"
    "v_pk_mul_f32 v[88:89], v[88:89], s[62:63]\n"
    "v_cvt_pk_bf16_f32 v190, v88, v89\n"
    "v_lshlrev_b32 v90, 16, v191\n"
    "v_and_b32 v91, 0xffff0000, v191\n"
    "v_pk_mul_f32 v[90:91], v[90:91], s[62:63]\n"
    "v_cvt_pk_bf16_f32 v191, v90, v91\n"
    "s_nop 1\n"
    "v_mfma_f32_16x16x32_bf16 v[8:11], a[64:67], v[176:179], 0\n"
    "v_sub_f32 v6, v6, v244\n"
    "v_sub_f32 v7, v7, v244\n"
    "v_mfma_f32_16x16x32_bf16 v[8:11], a[68:71], v[180:183], v[8:11]\n"
    "v_exp_f32 v4, v4\n"
    "v_mfma_f32_16x16x32_bf16 v[8:11], a[72:75], v[184:187], v[8:11]\n"
    "v_exp_f32 v5, v5\n"
    "v_mfma_f32_16x16x32_bf16 v[8:11], a[76:79], v[188:191], v[8:11]\n"
    "v_exp_f32 v6, v6\n"
    "v_mfma_f32_16x16x32_bf16 v[12:15], a[80:83], v[176:179], 0\n"
    "v_exp_f32 v7, v7\n"
    "v_mfma_f32_16x16x32_bf16 v[12:15], a[84:87], v[180:183], v[12:15]\n"
    "v_cvt_pk_bf16_f32 v2, v4, v5\n"
    "v_cvt_pk_bf16_f32 v3, v6, v7\n"
    "v_mfma_f32_16x16x32_bf16 v[12:15], a[88:91], v[184:187], v[12:15]\n"
    "v_mfma_f32_16x16x32_bf16 v[12:15], a[92:95], v[188:191], v[12:15]\n"
    "s_cmp_lt_u32 %11, 32\n"
    "s_cbranch_scc1 L2_m0_1x_%=\n"
    "L2_m0_1_%=:\n"
    "s_nop 5\n"
    "v_max3_f32 v245, v8, v9, v10\n"
    "v_max3_f32 v239, v11, v12, v13\n"
    "v_max3_f32 v245, v245, v14, v15\n"
    "v_max_f32 v245, v245, v239\n"
    "v_mov_b32 v239, v245\n"
    "s_nop 1\n"
    "v_permlane16_swap_b32 v245, v239\n"
    "s_nop 1\n"
    "v_max_f32 v245, v245, v239\n"
    "v_mov_b32 v239, v245\n"
    "s_nop 1\n"
    "v_permlane32_swap_b32 v245, v239\n"
    "s_nop 1\n"
    "v_max_f32 v245, v245, v239\n"
    "v_sub_f32 v72, 0, v245\n"
    "v_sub_f32 v73, 0, v245\n"
    "v_sub_f32 v74, 0, v245\n"
    "v_sub_f32 v75, 0, v245\n"
    "v_sub_f32 v76, 0, v245\n"
    "v_sub_f32 v77, 0, v245\n"
    "v_sub_f32 v78, 0, v245\n"
    "v_sub_f32 v79, 0, v245\n"
    "s_cmp_eq_u32 s66, 2\n"
    "s_cbranch_scc1 L2_m1_1x_%=\n"
    "L2_m1_1_%=:\n"
    "s_nop 1\n"
    "v_mfma_f32_16x16x32_bf16 v[40:43], a[160:163], v[176:179], v[72:75]\n"
    "v_sub_f32 v8, v8, v245\n"
    "v_sub_f32 v9, v9, v245\n"
    "v_mfma_f32_16x16x32_bf16 v[40:43], a[164:167], v[180:183], v[40:43]\n"
    "v_sub_f32 v10, v10, v245\n"
    "v_sub_f32 v11, v11, v245\n"
    "v_mfma_f32_16x16x32_bf16 v[40:43], a[168:171], v[184:187], v[40:43]\n"
    "v_exp_f32 v8, v8\n"
    "v_mfma_f32_16x16x32_bf16 v[40:43], a[172:175], v[188:191], v[40:43]\n"
    "v_exp_f32 v9, v9\n"
    "v_mfma_f32_16x16x32_bf16 v[44:47], a[176:179], v[176:179], v[76:79]\n"
    "v_exp_f32 v10, v10\n"
    "v_mfma_f32_16x16x32_bf16 v[44:47], a[180:183], v[180:183], v[44:47]\n"
    "v_exp_f32 v11, v11\n"
    "v_mfma_f32_16x16x32_bf16 v[44:47], a[184:187], v[184:187], v[44:47]\n"
    "v_cvt_pk_bf16_f32 v8, v8, v9\n"
    "v_cvt_pk_bf16_f32 v9, v10, v11\n"
    "v_mfma_f32_16x16x32_bf16 v[44:47], a[188:191], v[188:191], v[44:47]\n"
    "v_sub_f32 v12, v12, v245\n"
    "v_sub_f32 v13, v13, v245\n"
    "s_waitcnt vmcnt(8)\n"
    "s_barrier\n"
    "ds_read_b64_tr_b16 a[96:97], v225 offset:0\n"
    "ds_read_b64_tr_b16 a[98:99], v225 offset:4096\n"
    "ds_read_b64_tr_b16 a[100:101], v225 offset:256\n"
    "ds_read_b64_tr_b16 a[102:103], v225 offset:4352\n"
    "ds_read_b64_tr_b16 a[104:105], v225 offset:512\n"
    "ds_read_b64_tr_b16 a[106:107], v225 offset:4608\n"
    "ds_read_b64_tr_b16 a[108:109], v225 offset:768\n"
    "ds_read_b64_tr_b16 a[110:111], v225 offset:4864\n"
    "ds_read_b64_tr_b16 a[112:113], v225 offset:1024\n"
    "ds_read_b64_tr_b16 a[114:115], v225 offset:5120\n"
    "ds_read_b64_tr_b16 a[116:117], v225 offset:1280\n"
    "ds_read_b64_tr_b16 a[118:119], v225 offset:5376\n"
    "ds_read_b64_tr_b16 a[120:121], v225 offset:1536\n"
    "ds_read_b64_tr_b16 a[122:123], v225 offset:5632\n"
    "ds_read_b64_tr_b16 a[124:125], v225 offset:1792\n"
    "ds_read_b64_tr_b16 a[126:127], v225 offset:5888\n"
    "s_add_u32 m0, s54, s59\n"
    "v_sub_f32 v14, v14, v245\n"
    "v_sub_f32 v15, v15, v245\n"
    "v_exp_f32 v12, v12\n"
    "buffer_load_dwordx4 v228, s[36:39], s52 offen offset:0 nt lds\n"
    "v_exp_f32 v13, v13\n"
    "v_exp_f32 v14, v14\n"
    "v_exp_f32 v15, v15\n"
    "buffer_load_dwordx4 v229, s[36:39], s52 offen offset:1024 nt lds\n"
    "v_cvt_pk_bf16_f32 v10, v12, v13\n"
    "v_cvt_pk_bf16_f32 v11, v14, v15\n"
    "buffer_load_dwordx4 v230, s[36:39], s52 offen offset:2048 nt lds\n"
    "buffer_load_dwordx4 v231, s[36:39], s52 offen offset:3072 nt lds\n"
    "s_add_u32 s52, s52, 0x40000\n"
    "s_waitcnt lgkmcnt(0)\n"
    "s_sub_u32 s56, s56, 1\n"
    "s_cbranch_scc1 L2_ev0_%=\n"
    "s_branch L2_p1_%=\n"
    ".p2align 6\n"
    "L2_p0_%=:\n"
    "v_mfma_f32_16x16x32_bf16 v[96:99], a[96:99], v[32:35], v[96:99]\n"
    "s_waitcnt vmcnt(8)\n"
    "s_barrier\n"
    "v_mfma_f32_16x16x32_bf16 v[100:103], a[100:103], v[32:35], v[100:103]\n"
    "s_add_u32 m0, s54, s59\n"
    "v_exp_f32 v0, v0\n"
    "v_mfma_f32_16x16x32_bf16 v[104:107], a[104:107], v[32:35], v[104:107]\n"
    "v_exp_f32 v1, v1\n"
    "v_mfma_f32_16x16x32_bf16 v[108:111], a[108:111], v[32:35], v[108:111]\n"
    "ds_read_b128 a[64:67], v224 offset:0\n"
    "v_cvt_pk_bf16_f32 v0, v0, v1\n"
    "v_mfma_f32_16x16x32_bf16 v[112:115], a[112:115], v[32:35], v[112:115]\n"
    "v_exp_f32 v2, v2\n"
    "v_mfma_f32_16x16x32_bf16 v[116:119], a[116:119], v[32:35], v[116:119]\n"
    "ds_read_b128 a[68:71], v224 offset:512\n"
    "ds_read_b128 a[72:75], v224 offset:1024\n"
    "v_mfma_f32_16x16x32_bf16 v[120:123], a[120:123], v[32:35], v[120:123]\n"
    "v_exp_f32 v3, v3\n"
    "v_mfma_f32_16x16x32_bf16 v[124:127], a[124:127], v[32:35], v[124:127]\n"
    "ds_read_b128 a[76:79], v224 offset:1536\n"
    "v_cvt_pk_bf16_f32 v1, v2, v3\n"
    "v_mfma_f32_16x16x32_bf16 a[128:131], a[144:147], v[32:35], a[128:131]\n"
    "buffer_load_dwordx4 v228, s[36:39], s52 offen offset:0 lds\n"
    "v_exp_f32 v4, v4\n"
    "v_mfma_f32_16x16x32_bf16 v[128:131], a[96:99], v[40:43], v[128:131]\n"
    "ds_read_b128 a[80:83], v224 offset:4096\n"
    "ds_read_b128 a[84:87], v224 offset:4608\n"
    "v_mfma_f32_16x16x32_bf16 v[132:135], a[100:103], v[40:43], v[132:135]\n"
    "buffer_load_dwordx4 v229, s[36:39], s52 offen offset:1024 lds\n"
    "v_exp_f32 v5, v5\n"
    "v_mfma_f32_16x16x32_bf16 v[136:139], a[104:107], v[40:43], v[136:139]\n"
    "ds_read_b128 a[88:91], v224 offset:5120\n"
    "ds_read_b128 a[92:95], v224 offset:5632\n"
    "v_mfma_f32_16x16x32_bf16 v[140:143], a[108:111], v[40:43], v[140:143]\n"
    "buffer_load_dwordx4 v230, s[36:39], s52 offen offset:2048 lds\n"
    "v_exp_f32 v6, v6\n"
    "v_mfma_f32_16x16x32_bf16 v[144:147], a[112:115], v[40:43], v[144:147]\n"
    "ds_read_b64_tr_b16 a[96:97], v225 offset:0\n"
    "v_cvt_pk_bf16_f32 v2, v4, v5\n"
    "v_mfma_f32_16x16x32_bf16 v[148:151], a[116:119], v[40:43], v[148:151]\n"
    "ds_read_b64_tr_b16 a[98:99], v225 offset:4096\n"
    "buffer_load_dwordx4 v231, s[36:39], s52 offen offset:3072 lds\n"
    "v_exp_f32 v7, v7\n"
    "v_mfma_f32_16x16x32_bf16 v[152:155], a[120:123], v[40:43], v[152:155]\n"
    "s_add_u32 s52, s52, 0x40000\n"
    "ds_read_b64_tr_b16 a[100:101], v225 offset:256\n"
    "ds_read_b64_tr_b16 a[102:103], v225 offset:4352\n"
    "v_mfma_f32_16x16x32_bf16 v[156:159], a[124:127], v[40:43], v[156:159]\n"
    "ds_read_b64_tr_b16 a[104:105], v225 offset:512\n"
    "v_cvt_pk_bf16_f32 v3, v6, v7\n"
    "v_mfma_f32_16x16x32_bf16 a[132:135], a[144:147], v[40:43], a[132:135]\n"
    "s_waitcnt lgkmcnt(5)\n"
    "v_exp_f32 v8, v8\n"
    "v_mfma_f32_16x16x32_bf16 v[32:35], a[64:67], v[160:163], v[64:67]\n"
    "ds_read_b64_tr_b16 a[106:107], v225 offset:4608\n"
    "ds_read_b64_tr_b16 a[108:109], v225 offset:768\n"
    "v_mfma_f32_16x16x32_bf16 v[32:35], a[68:71], v[164:167], v[32:35]\n"
    "v_exp_f32 v9, v9\n"
    "v_mfma_f32_16x16x32_bf16 v[32:35], a[72:75], v[168:171], v[32:35]\n"
    "ds_read_b64_tr_b16 a[110:111], v225 offset:4864\n"
    "v_cvt_pk_bf16_f32 v8, v8, v9\n"
    "v_mfma_f32_16x16x32_bf16 v[32:35], a[76:79], v[172:175], v[32:35]\n"
    "v_exp_f32 v10, v10\n"
    "v_mfma_f32_16x16x32_bf16 v[36:39], a[80:83], v[160:163], v[68:71]\n"
    "ds_read_b64_tr_b16 a[112:113], v225 offset:1024\n"
    "ds_read_b64_tr_b16 a[114:115], v225 offset:5120\n"
    "v_mfma_f32_16x16x32_bf16 v[36:39], a[84:87], v[164:167], v[36:39]\n"
    "v_exp_f32 v11, v11\n"
    "v_mfma_f32_16x16x32_bf16 v[36:39], a[88:91], v[168:171], v[36:39]\n"
    "ds_read_b64_tr_b16 a[116:117], v225 offset:1280\n"
    "ds_read_b64_tr_b16 a[118:119], v225 offset:5376\n"
    "v_mfma_f32_16x16x32_bf16 v[36:39], a[92:95], v[172:175], v[36:39]\n"
    "v_exp_f32 v12, v12\n"
    "v_mfma_f32_16x16x32_bf16 v[40:43], a[64:67], v[176:179], v[72:75]\n"
    "ds_read_b64_tr_b16 a[120:121], v225 offset:1536\n"
    "v_cvt_pk_bf16_f32 v9, v10, v11\n"
    "v_mfma_f32_16x16x32_bf16 v[40:43], a[68:71], v[180:183], v[40:43]\n"
    "ds_read_b64_tr_b16 a[122:123], v225 offset:5632\n"
    "v_exp_f32 v13, v13\n"
    "v_mfma_f32_16x16x32_bf16 v[40:43], a[72:75], v[184:187], v[40:43]\n"
    "ds_read_b64_tr_b16 a[124:125], v225 offset:1792\n"
    "ds_read_b64_tr_b16 a[126:127], v225 offset:5888\n"
    "v_mfma_f32_16x16x32_bf16 v[40:43], a[76:79], v[188:191], v[40:43]\n"
    "v_exp_f32 v14, v14\n"
    "v_mfma_f32_16x16x32_bf16 v[44:47], a[80:83], v[176:179], v[76:79]\n"
    "s_sub_u32 s56, s56, 1\n"
    "v_cvt_pk_bf16_f32 v10, v12, v13\n"
    "v_mfma_f32_16x16x32_bf16 v[44:47], a[84:87], v[180:183], v[44:47]\n"
    "v_exp_f32 v15, v15\n"
    "v_mfma_f32_16x16x32_bf16 v[44:47], a[88:91], v[184:187], v[44:47]\n"
    "v_cvt_pk_bf16_f32 v11, v14, v15\n"
    "v_mfma_f32_16x16x32_bf16 v[44:47], a[92:95], v[188:191], v[44:47]\n"
    "s_waitcnt lgkmcnt(0)\n"
    "s_cbranch_scc1 L2_ev0_%=\n"
    "L2_p1_%=:\n"
    "v_mfma_f32_16x16x32_bf16 v[96:99], a[96:99], v[0:3], v[96:99]\n"
    "v_exp_f32 v32, v32\n"
    "v_mfma_f32_16x16x32_bf16 v[100:103], a[100:103], v[0:3], v[100:103]\n"
    "s_add_u32 m0, s55, s59\n"
    "ds_read_b128 a[64:67], v224 offset:8192\n"
    "v_mfma_f32_16x16x32_bf16 v[104:107], a[104:107], v[0:3], v[104:107]\n"
    "v_exp_f32 v33, v33\n"
    "v_mfma_f32_16x16x32_bf16 v[108:111], a[108:111], v[0:3], v[108:111]\n"
    "ds_read_b128 a[68:71], v224 offset:8704\n"
    "v_cvt_pk_bf16_f32 v32, v32, v33\n"
    "v_mfma_f32_16x16x32_bf16 v[112:115], a[112:115], v[0:3], v[112:115]\n"
    "v_exp_f32 v34, v34\n"
    "v_mfma_f32_16x16x32_bf16 v[116:119], a[116:119], v[0:3], v[116:119]\n"
    "ds_read_b128 a[72:75], v224 offset:9216\n"
    "ds_read_b128 a[76:79], v224 offset:9728\n"
    "v_mfma_f32_16x16x32_bf16 v[120:123], a[120:123], v[0:3], v[120:123]\n"
    "s_mov_b32 s59, s58\n"
    "v_exp_f32 v35, v35\n"
    "v_mfma_f32_16x16x32_bf16 v[124:127], a[124:127], v[0:3], v[124:127]\n"
    "s_add_u32 s58, s58, 32768\n"
    "buffer_load_dwordx4 v232, s[40:43], s53 offen offset:0 lds\n"
    "v_mfma_f32_16x16x32_bf16 a[128:131], a[144:147], v[0:3], a[128:131]\n"
    "s_cmp_eq_u32 s58, 98304\n"
    "s_cselect_b32 s58, 0, s58\n"
    "v_exp_f32 v36, v36\n"
    "v_mfma_f32_16x16x32_bf16 v[128:131], a[96:99], v[8:11], v[128:131]\n"
    "ds_read_b128 a[80:83], v224 offset:12288\n"
    "v_cvt_pk_bf16_f32 v33, v34, v35\n"
    "v_mfma_f32_16x16x32_bf16 v[132:135], a[100:103], v[8:11], v[132:135]\n"
    "ds_read_b128 a[84:87], v224 offset:12800\n"
    "buffer_load_dwordx4 v233, s[40:43], s53 offen offset:1024 lds\n"
    "v_exp_f32 v37, v37\n"
    "v_mfma_f32_16x16x32_bf16 v[136:139], a[104:107], v[8:11], v[136:139]\n"
    "ds_read_b128 a[88:91], v224 offset:13312\n"
    "ds_read_b128 a[92:95], v224 offset:13824\n"
    "v_mfma_f32_16x16x32_bf16 v[140:143], a[108:111], v[8:11], v[140:143]\n"
    "buffer_load_dwordx4 v234, s[40:43], s53 offen offset:2048 lds\n"
    "v_exp_f32 v38, v38\n"
    "v_mfma_f32_16x16x32_bf16 v[144:147], a[112:115], v[8:11], v[144:147]\n"
    "ds_read_b64_tr_b16 a[96:97], v225 offset:8192\n"
    "v_cvt_pk_bf16_f32 v34, v36, v37\n"
    "v_mfma_f32_16x16x32_bf16 v[148:151], a[116:119], v[8:11], v[148:151]\n"
    "ds_read_b64_tr_b16 a[98:99], v225 offset:12288\n"
    "buffer_load_dwordx4 v235, s[40:43], s53 offen offset:3072 lds\n"
    "v_exp_f32 v39, v39\n"
    "v_mfma_f32_16x16x32_bf16 v[152:155], a[120:123], v[8:11], v[152:155]\n"
    "s_add_u32 s53, s53, 0x40000\n"
    "ds_read_b64_tr_b16 a[100:101], v225 offset:8448\n"
    "ds_read_b64_tr_b16 a[102:103], v225 offset:12544\n"
    "v_mfma_f32_16x16x32_bf16 v[156:159], a[124:127], v[8:11], v[156:159]\n"
    "ds_read_b64_tr_b16 a[104:105], v225 offset:8704\n"
    "v_cvt_pk_bf16_f32 v35, v38, v39\n"
    "v_mfma_f32_16x16x32_bf16 a[132:135], a[144:147], v[8:11], a[132:135]\n"
    "s_waitcnt lgkmcnt(5)\n"
    "v_exp_f32 v40, v40\n"
    "v_mfma_f32_16x16x32_bf16 v[0:3], a[64:67], v[160:163], v[64:67]\n"
    "ds_read_b64_tr_b16 a[106:107], v225 offset:12800\n"
    "ds_read_b64_tr_b16 a[108:109], v225 offset:8960\n"
    "v_mfma_f32_16x16x32_bf16 v[0:3], a[68:71], v[164:167], v[0:3]\n"
    "v_exp_f32 v41, v41\n"
    "v_mfma_f32_16x16x32_bf16 v[0:3], a[72:75], v[168:171], v[0:3]\n"
    "ds_read_b64_tr_b16 a[110:111], v225 offset:13056\n"
    "v_cvt_pk_bf16_f32 v40, v40, v41\n"
    "v_mfma_f32_16x16x32_bf16 v[0:3], a[76:79], v[172:175], v[0:3]\n"
    "v_exp_f32 v42, v42\n"
    "v_mfma_f32_16x16x32_bf16 v[4:7], a[80:83], v[160:163], v[68:71]\n"
    "ds_read_b64_tr_b16 a[112:113], v225 offset:9216\n"
    "ds_read_b64_tr_b16 a[114:115], v225 offset:13312\n"
    "v_mfma_f32_16x16x32_bf16 v[4:7], a[84:87], v[164:167], v[4:7]\n"
    "v_exp_f32 v43, v43\n"
    "v_mfma_f32_16x16x32_bf16 v[4:7], a[88:91], v[168:171], v[4:7]\n"
    "ds_read_b64_tr_b16 a[116:117], v225 offset:9472\n"
    "ds_read_b64_tr_b16 a[118:119], v225 offset:13568\n"
    "v_mfma_f32_16x16x32_bf16 v[4:7], a[92:95], v[172:175], v[4:7]\n"
    "v_exp_f32 v44, v44\n"
    "v_mfma_f32_16x16x32_bf16 v[8:11], a[64:67], v[176:179], v[72:75]\n"
    "ds_read_b64_tr_b16 a[120:121], v225 offset:9728\n"
    "v_cvt_pk_bf16_f32 v41, v42, v43\n"
    "v_mfma_f32_16x16x32_bf16 v[8:11], a[68:71], v[180:183], v[8:11]\n"
    "ds_read_b64_tr_b16 a[122:123], v225 offset:13824\n"
    "v_exp_f32 v45, v45\n"
    "v_mfma_f32_16x16x32_bf16 v[8:11], a[72:75], v[184:187], v[8:11]\n"
    "ds_read_b64_tr_b16 a[124:125], v225 offset:9984\n"
    "ds_read_b64_tr_b16 a[126:127], v225 offset:14080\n"
    "v_mfma_f32_16x16x32_bf16 v[8:11], a[76:79], v[188:191], v[8:11]\n"
    "v_exp_f32 v46, v46\n"
    "v_mfma_f32_16x16x32_bf16 v[12:15], a[80:83], v[176:179], v[76:79]\n"
    "s_sub_u32 s56, s56, 1\n"
    "v_cvt_pk_bf16_f32 v42, v44, v45\n"
    "v_add_u32 v224, s58, v226\n"
    "v_mfma_f32_16x16x32_bf16 v[12:15], a[84:87], v[180:183], v[12:15]\n"
    "v_exp_f32 v47, v47\n"
    "v_mfma_f32_16x16x32_bf16 v[12:15], a[88:91], v[184:187], v[12:15]\n"
    "v_cvt_pk_bf16_f32 v43, v46, v47\n"
    "v_add_u32 v225, s58, v227\n"
    "v_mfma_f32_16x16x32_bf16 v[12:15], a[92:95], v[188:191], v[12:15]\n"
    "s_waitcnt lgkmcnt(0)\n"
    "s_cbranch_scc0 L2_p0_%=\n"
    "s_cmp_eq_u32 s57, 0\n"
    "s_cbranch_scc0 L2_fix1_%=\n"
    "L2_tail0_%=:\n"
    "v_mfma_f32_16x16x32_bf16 v[96:99], a[96:99], v[32:35], v[96:99]\n"
    "s_waitcnt vmcnt(8)\n"
    "s_barrier\n"
    "v_mfma_f32_16x16x32_bf16 v[100:103], a[100:103], v[32:35], v[100:103]\n"
    "s_mov_b32 s61, 0x5f800000\n"
    "ds_read_b64_tr_b16 a[64:65], v225 offset:0\n"
    "ds_read_b64_tr_b16 a[66:67], v225 offset:4096\n"
    "v_mfma_f32_16x16x32_bf16 v[104:107], a[104:107], v[32:35], v[104:107]\n"
    "s_mov_b64 s[76:77], 0\n"
    "v_exp_f32 v0, v0\n"
    "v_mfma_f32_16x16x32_bf16 v[108:111], a[108:111], v[32:35], v[108:111]\n"
    "ds_read_b64_tr_b16 a[68:69], v225 offset:256\n"
    "ds_read_b64_tr_b16 a[70:71], v225 offset:4352\n"
    "v_mfma_f32_16x16x32_bf16 v[112:115], a[112:115], v[32:35], v[112:115]\n"
    "v_exp_f32 v1, v1\n"
    "v_mfma_f32_16x16x32_bf16 v[116:119], a[116:119], v[32:35], v[116:119]\n"
    "ds_read_b64_tr_b16 a[72:73], v225 offset:512\n"
    "ds_read_b64_tr_b16 a[74:75], v225 offset:4608\n"
    "v_mfma_f32_16x16x32_bf16 v[120:123], a[120:123], v[32:35], v[120:123]\n"
    "v_exp_f32 v2, v2\n"
    "v_mfma_f32_16x16x32_bf16 v[124:127], a[124:127], v[32:35], v[124:127]\n"
    "ds_read_b64_tr_b16 a[76:77], v225 offset:768\n"
    "ds_read_b64_tr_b16 a[78:79], v225 offset:4864\n"
    "v_mfma_f32_16x16x32_bf16 a[128:131], a[144:147], v[32:35], a[128:131]\n"
    "v_exp_f32 v3, v3\n"
    "v_mfma_f32_16x16x32_bf16 v[128:131], a[96:99], v[40:43], v[128:131]\n"
    "ds_read_b64_tr_b16 a[80:81], v225 offset:1024\n"
    "ds_read_b64_tr_b16 a[82:83], v225 offset:5120\n"
    "v_mfma_f32_16x16x32_bf16 v[132:135], a[100:103], v[40:43], v[132:135]\n"
    "v_cvt_pk_bf16_f32 v0, v0, v1\n"
    "v_cvt_pk_bf16_f32 v1, v2, v3\n"
    "v_mfma_f32_16x16x32_bf16 v[136:139], a[104:107], v[40:43], v[136:139]\n"
    "ds_read_b64_tr_b16 a[84:85], v225 offset:1280\n"
    "ds_read_b64_tr_b16 a[86:87], v225 offset:5376\n"
    "v_mfma_f32_16x16x32_bf16 v[140:143], a[108:111], v[40:43], v[140:143]\n"
    "v_exp_f32 v4, v4\n"
    "v_mfma_f32_16x16x32_bf16 v[144:147], a[112:115], v[40:43], v[144:147]\n"
    "ds_read_b64_tr_b16 a[88:89], v225 offset:1536\n"
    "ds_read_b64_tr_b16 a[90:91], v225 offset:5632\n"
    "v_mfma_f32_16x16x32_bf16 v[148:151], a[116:119], v[40:43], v[148:151]\n"
    "v_exp_f32 v5, v5\n"
    "v_mfma_f32_16x16x32_bf16 v[152:155], a[120:123], v[40:43], v[152:155]\n"
    "ds_read_b64_tr_b16 a[92:93], v225 offset:1792\n"
    "ds_read_b64_tr_b16 a[94:95], v225 offset:5888\n"
    "v_mfma_f32_16x16x32_bf16 v[156:159], a[124:127], v[40:43], v[156:159]\n"
    "v_exp_f32 v6, v6\n"
    "v_mfma_f32_16x16x32_bf16 a[132:135], a[144:147], v[40:43], a[132:135]\n"
    "v_exp_f32 v7, v7\n"
    "s_waitcnt lgkmcnt(0)\n"
    "s_nop 0\n"
    "v_cvt_pk_bf16_f32 v2, v4, v5\n"
    "s_nop 0\n"
    "v_cvt_pk_bf16_f32 v3, v6, v7\n"
    "s_nop 1\n"
    "v_mfma_f32_16x16x32_bf16 a[128:131], a[144:147], v[0:3], a[128:131]\n"
    "v_exp_f32 v8, v8\n"
    "v_mfma_f32_16x16x32_bf16 v[96:99], a[64:67], v[0:3], v[96:99]\n"
    "v_exp_f32 v9, v9\n"
    "v_mfma_f32_16x16x32_bf16 v[100:103], a[68:71], v[0:3], v[100:103]\n"
    "v_exp_f32 v10, v10\n"
    "v_mfma_f32_16x16x32_bf16 v[104:107], a[72:75], v[0:3], v[104:107]\n"
    "v_exp_f32 v11, v11\n"
    "v_mfma_f32_16x16x32_bf16 v[108:111], a[76:79], v[0:3], v[108:111]\n"
    "v_cvt_pk_bf16_f32 v8, v8, v9\n"
    "v_cvt_pk_bf16_f32 v9, v10, v11\n"
    "v_mfma_f32_16x16x32_bf16 v[112:115], a[80:83], v[0:3], v[112:115]\n"
    "v_exp_f32 v12, v12\n"
    "v_mfma_f32_16x16x32_bf16 v[116:119], a[84:87], v[0:3], v[116:119]\n"
    "v_exp_f32 v13, v13\n"
    "v_mfma_f32_16x16x32_bf16 v[120:123], a[88:91], v[0:3], v[120:123]\n"
    "v_exp_f32 v14, v14\n"
    "v_mfma_f32_16x16x32_bf16 v[124:127], a[92:95], v[0:3], v[124:127]\n"
    "v_exp_f32 v15, v15\n"
    "s_nop 0\n"
    "v_cvt_pk_bf16_f32 v10, v12, v13\n"
    "s_nop 0\n"
    "v_cvt_pk_bf16_f32 v11, v14, v15\n"
    "s_nop 1\n"
    "v_mfma_f32_16x16x32_bf16 a[132:135], a[144:147], v[8:11], a[132:135]\n"
    "v_accvgpr_read_b32 v88, a128\n"
    "v_cmp_nge_f32 vcc, s61, v88\n"
    "v_mfma_f32_16x16x32_bf16 v[128:131], a[64:67], v[8:11], v[128:131]\n"
    "v_rcp_f32 v64, v88\n"
    "v_mfma_f32_16x16x32_bf16 v[132:135], a[68:71], v[8:11], v[132:135]\n"
    "s_or_b64 s[76:77], s[76:77], vcc\n"
    "v_mov_b32 v65, v64\n"
    "v_mfma_f32_16x16x32_bf16 v[136:139], a[72:75], v[8:11], v[136:139]\n"
    "v_mul_f32 v96, v96, v64\n"
    "v_mul_f32 v97, v97, v64\n"
    "v_mfma_f32_16x16x32_bf16 v[140:143], a[76:79], v[8:11], v[140:143]\n"
    "v_mul_f32 v98, v98, v64\n"
    "v_mul_f32 v99, v99, v64\n"
    "v_mfma_f32_16x16x32_bf16 v[144:147], a[80:83], v[8:11], v[144:147]\n"
    "v_cvt_pk_bf16_f32 v96, v96, v97\n"
    "v_cvt_pk_bf16_f32 v97, v98, v99\n"
    "v_mfma_f32_16x16x32_bf16 v[148:151], a[84:87], v[8:11], v[148:151]\n"
    "ds_write_b64 v251, v[96:97] offset:0\n"
    "v_mul_f32 v100, v100, v64\n"
    "v_mfma_f32_16x16x32_bf16 v[152:155], a[88:91], v[8:11], v[152:155]\n"
    "v_mul_f32 v101, v101, v64\n"
    "v_mul_f32 v102, v102, v64\n"
    "v_mfma_f32_16x16x32_bf16 v[156:159], a[92:95], v[8:11], v[156:159]\n"
    "v_mul_f32 v103, v103, v64\n"
    "v_cvt_pk_bf16_f32 v100, v100, v101\n"
    "v_cvt_pk_bf16_f32 v101, v102, v103\n"
    "v_pk_mul_f32 v[104:105], v[104:105], v[64:65]\n"
    "v_pk_mul_f32 v[106:107], v[106:107], v[64:65]\n"
    "v_cvt_pk_bf16_f32 v104, v104, v105\n"
    "v_cvt_pk_bf16_f32 v105, v106, v107\n"
    "v_pk_mul_f32 v[108:109], v[108:109], v[64:65]\n"
    "v_pk_mul_f32 v[110:111], v[110:111], v[64:65]\n"
    "v_cvt_pk_bf16_f32 v108, v108, v109\n"
    "v_cvt_pk_bf16_f32 v109, v110, v111\n"
    "ds_write_b64 v251, v[100:101] offset:32\n"
    "ds_write_b64 v251, v[104:105] offset:64\n"
    "ds_write_b64 v251, v[108:109] offset:96\n"
    "v_pk_mul_f32 v[112:113], v[112:113], v[64:65]\n"
    "v_pk_mul_f32 v[114:115], v[114:115], v[64:65]\n"
    "v_cvt_pk_bf16_f32 v112, v112, v113\n"
    "v_cvt_pk_bf16_f32 v113, v114, v115\n"
    "v_pk_mul_f32 v[116:117], v[116:117], v[64:65]\n"
    "v_pk_mul_f32 v[118:119], v[118:119], v[64:65]\n"
    "v_cvt_pk_bf16_f32 v116, v116, v117\n"
    "v_cvt_pk_bf16_f32 v117, v118, v119\n"
    "ds_write_b64 v251, v[112:113] offset:128\n"
    "ds_write_b64 v251, v[116:117] offset:160\n"
    "v_pk_mul_f32 v[120:121], v[120:121], v[64:65]\n"
    "v_pk_mul_f32 v[122:123], v[122:123], v[64:65]\n"
    "v_cvt_pk_bf16_f32 v120, v120, v121\n"
    "v_cvt_pk_bf16_f32 v121, v122, v123\n"
    "v_pk_mul_f32 v[124:125], v[124:125], v[64:65]\n"
    "v_pk_mul_f32 v[126:127], v[126:127], v[64:65]\n"
    "v_cvt_pk_bf16_f32 v124, v124, v125\n"
    "v_cvt_pk_bf16_f32 v125, v126, v127\n"
    "ds_write_b64 v251, v[120:121] offset:192\n"
    "ds_write_b64 v251, v[124:125] offset:224\n"
    "ds_read_b128 v[32:35], v252 offset:0\n"
    "ds_read_b128 v[36:39], v252 offset:1088\n"
    "ds_read_b128 v[40:43], v252 offset:2176\n"
    "ds_read_b128 v[44:47], v252 offset:3264\n"
    "v_accvgpr_read_b32 v89, a132\n"
    "v_cmp_nge_f32 vcc, s61, v89\n"
    "v_rcp_f32 v66, v89\n"
    "s_or_b64 s[76:77], s[76:77], vcc\n"
    "v_mov_b32 v67, v66\n"
    "v_pk_mul_f32 v[128:129], v[128:129], v[66:67]\n"
    "v_pk_mul_f32 v[130:131], v[130:131], v[66:67]\n"
    "v_cvt_pk_bf16_f32 v128, v128, v129\n"
    "v_cvt_pk_bf16_f32 v129, v130, v131\n"
    "v_pk_mul_f32 v[132:133], v[132:133], v[66:67]\n"
    "v_pk_mul_f32 v[134:135], v[134:135], v[66:67]\n"
    "v_cvt_pk_bf16_f32 v132, v132, v133\n"
    "v_cvt_pk_bf16_f32 v133, v134, v135\n"
    "s_mov_b32 s53, 0\n"
    "s_waitcnt lgkmcnt(3)\n"
    "buffer_store_dwordx4 v[32:35], v253, s[48:51], s53 offen nt\n"
    "ds_write_b64 v251, v[128:129] offset:4352\n"
    "ds_write_b64 v251, v[132:133] offset:4384\n"
    "v_pk_mul_f32 v[136:137], v[136:137], v[66:67]\n"
    "v_pk_mul_f32 v[138:139], v[138:139], v[66:67]\n"
    "v_cvt_pk_bf16_f32 v136, v136, v137\n"
    "v_cvt_pk_bf16_f32 v137, v138, v139\n"
    "v_pk_mul_f32 v[140:141], v[140:141], v[66:67]\n"
    "v_pk_mul_f32 v[142:143], v[142:143], v[66:67]\n"
    "v_cvt_pk_bf16_f32 v140, v140, v141\n"
    "v_cvt_pk_bf16_f32 v141, v142, v143\n"
    "s_mov_b32 s53, 16384\n"
    "s_waitcnt lgkmcnt(4)\n"
    "buffer_store_dwordx4 v[36:39], v253, s[48:51], s53 offen nt\n"
    "ds_write_b64 v251, v[136:137] offset:4416\n"
    "ds_write_b64 v251, v[140:141] offset:4448\n"
    "v_pk_mul_f32 v[144:145], v[144:145], v[66:67]\n"
    "v_pk_mul_f32 v[146:147], v[146:147], v[66:67]\n"
    "v_cvt_pk_bf16_f32 v144, v144, v145\n"
    "v_cvt_pk_bf16_f32 v145, v146, v147\n"
    "v_pk_mul_f32 v[148:149], v[148:149], v[66:67]\n"
    "v_pk_mul_f32 v[150:151], v[150:151], v[66:67]\n"
    "v_cvt_pk_bf16_f32 v148, v148, v149\n"
    "v_cvt_pk_bf16_f32 v149, v150, v151\n"
    "s_mov_b32 s53, 32768\n"
    "s_waitcnt lgkmcnt(5)\n"
    "buffer_store_dwordx4 v[40:43], v253, s[48:51], s53 offen nt\n"
    "ds_write_b64 v251, v[144:145] offset:4480\n"
    "ds_write_b64 v251, v[148:149] offset:4512\n"
    "v_pk_mul_f32 v[152:153], v[152:153], v[66:67]\n"
    "v_pk_mul_f32 v[154:155], v[154:155], v[66:67]\n"
    "v_cvt_pk_bf16_f32 v152, v152, v153\n"
    "v_cvt_pk_bf16_f32 v153, v154, v155\n"
    "v_pk_mul_f32 v[156:157], v[156:157], v[66:67]\n"
    "v_pk_mul_f32 v[158:159], v[158:159], v[66:67]\n"
    "v_cvt_pk_bf16_f32 v156, v156, v157\n"
    "v_cvt_pk_bf16_f32 v157, v158, v159\n"
    "s_mov_b32 s53, 49152\n"
    "s_waitcnt lgkmcnt(6)\n"
    "buffer_store_dwordx4 v[44:47], v253, s[48:51], s53 offen nt\n"
    "ds_write_b64 v251, v[152:153] offset:4544\n"
    "ds_write_b64 v251, v[156:157] offset:4576\n"
    "ds_read_b128 v[48:51], v252 offset:4352\n"
    "ds_read_b128 v[52:55], v252 offset:5440\n"
    "ds_read_b128 v[56:59], v252 offset:6528\n"
    "ds_read_b128 v[60:63], v252 offset:7616\n"
    "s_mov_b32 s53, 65536\n"
    "s_waitcnt lgkmcnt(3)\n"
    "buffer_store_dwordx4 v[48:51], v253, s[48:51], s53 offen nt\n"
    "s_mov_b32 s53, 81920\n"
    "s_waitcnt lgkmcnt(2)\n"
    "buffer_store_dwordx4 v[52:55], v253, s[48:51], s53 offen nt\n"
    "s_mov_b32 s53, 98304\n"
    "s_waitcnt lgkmcnt(1)\n"
    "buffer_store_dwordx4 v[56:59], v253, s[48:51], s53 offen nt\n"
    "s_mov_b32 s53, 114688\n"
    "s_waitcnt lgkmcnt(0)\n"
    "buffer_store_dwordx4 v[60:63], v253, s[48:51], s53 offen nt\n"
    "s_or_b32 %0, s76, s77\n"
    "s_branch L2_end_%=\n"
    "L2_m0_0x_%=:\n"
    "s_nop 7\n"
    "v_cmp_ge_i32 s[68:69], 0, v250\n"
    "v_cmp_ge_i32 s[70:71], 1, v250\n"
    "v_cmp_ge_i32 s[72:73], 2, v250\n"
    "v_cmp_ge_i32 s[74:75], 3, v250\n"
    "s_nop 1\n"
    "v_cndmask_b32 v0, v0, v249, s[68:69]\n"
    "v_cndmask_b32 v1, v1, v249, s[70:71]\n"
    "v_cndmask_b32 v2, v2, v249, s[72:73]\n"
    "v_cndmask_b32 v3, v3, v249, s[74:75]\n"
    "v_cmp_ge_i32 s[68:69], 16, v250\n"
    "v_cmp_ge_i32 s[70:71], 17, v250\n"
    "v_cmp_ge_i32 s[72:73], 18, v250\n"
    "v_cmp_ge_i32 s[74:75], 19, v250\n"
    "s_nop 1\n"
    "v_cndmask_b32 v4, v4, v249, s[68:69]\n"
    "v_cndmask_b32 v5, v5, v249, s[70:71]\n"
    "v_cndmask_b32 v6, v6, v249, s[72:73]\n"
    "v_cndmask_b32 v7, v7, v249, s[74:75]\n"
    "s_branch L2_m0_0_%=\n"
    "L2_m1_0x_%=:\n"
    "v_cmp_ge_i32 s[68:69], 0, v248\n"
    "v_cmp_ge_i32 s[70:71], 1, v248\n"
    "v_cmp_ge_i32 s[72:73], 2, v248\n"
    "v_cmp_ge_i32 s[74:75], 3, v248\n"
    "s_nop 1\n"
    "v_cndmask_b32 v64, v64, v249, s[68:69]\n"
    "v_cndmask_b32 v65, v65, v249, s[70:71]\n"
    "v_cndmask_b32 v66, v66, v249, s[72:73]\n"
    "v_cndmask_b32 v67, v67, v249, s[74:75]\n"
    "v_cmp_ge_i32 s[68:69], 16, v248\n"
    "v_cmp_ge_i32 s[70:71], 17, v248\n"
    "v_cmp_ge_i32 s[72:73], 18, v248\n"
    "v_cmp_ge_i32 s[74:75], 19, v248\n"
    "s_nop 1\n"
    "v_cndmask_b32 v68, v68, v249, s[68:69]\n"
    "v_cndmask_b32 v69, v69, v249, s[70:71]\n"
    "v_cndmask_b32 v70, v70, v249, s[72:73]\n"
    "v_cndmask_b32 v71, v71, v249, s[74:75]\n"
    "s_branch L2_m1_0_%=\n"
    "L2_m0_1x_%=:\n"
    "s_nop 7\n"
    "v_cmp_ge_i32 s[68:69], 0, v250\n"
    "v_cmp_ge_i32 s[70:71], 1, v250\n"
    "v_cmp_ge_i32 s[72:73], 2, v250\n"
    "v_cmp_ge_i32 s[74:75], 3, v250\n"
    "s_nop 1\n"
    "v_cndmask_b32 v8, v8, v249, s[68:69]\n"
    "v_cndmask_b32 v9, v9, v249, s[70:71]\n"
    "v_cndmask_b32 v10, v10, v249, s[72:73]\n"
    "v_cndmask_b32 v11, v11, v249, s[74:75]\n"
    "v_cmp_ge_i32 s[68:69], 16, v250\n"
    "v_cmp_ge_i32 s[70:71], 17, v250\n"
    "v_cmp_ge_i32 s[72:73], 18, v250\n"
    "v_cmp_ge_i32 s[74:75], 19, v250\n"
    "s_nop 1\n"
    "v_cndmask_b32 v12, v12, v249, s[68:69]\n"
    "v_cndmask_b32 v13, v13, v249, s[70:71]\n"
    "v_cndmask_b32 v14, v14, v249, s[72:73]\n"
    "v_cndmask_b32 v15, v15, v249, s[74:75]\n"
    "s_branch L2_m0_1_%=\n"
    "L2_m1_1x_%=:\n"
    "v_cmp_ge_i32 s[68:69], 0, v248\n"
    "v_cmp_ge_i32 s[70:71], 1, v248\n"
    "v_cmp_ge_i32 s[72:73], 2, v248\n"
    "v_cmp_ge_i32 s[74:75], 3, v248\n"
    "s_nop 1\n"
    "v_cndmask_b32 v72, v72, v249, s[68:69]\n"
    "v_cndmask_b32 v73, v73, v249, s[70:71]\n"
    "v_cndmask_b32 v74, v74, v249, s[72:73]\n"
    "v_cndmask_b32 v75, v75, v249, s[74:75]\n"
    "v_cmp_ge_i32 s[68:69], 16, v248\n"
    "v_cmp_ge_i32 s[70:71], 17, v248\n"
    "v_cmp_ge_i32 s[72:73], 18, v248\n"
    "v_cmp_ge_i32 s[74:75], 19, v248\n"
    "s_nop 1\n"
    "v_cndmask_b32 v76, v76, v249, s[68:69]\n"
    "v_cndmask_b32 v77, v77, v249, s[70:71]\n"
    "v_cndmask_b32 v78, v78, v249, s[72:73]\n"
    "v_cndmask_b32 v79, v79, v249, s[74:75]\n"
    "s_branch L2_m1_1_%=\n"
    "L2_fix0_%=:\n"
    "v_cmp_ge_i32 s[68:69], 0, v248\n"
    "v_cmp_ge_i32 s[70:71], 1, v248\n"
    "v_cmp_ge_i32 s[72:73], 2, v248\n"
    "v_cmp_ge_i32 s[74:75], 3, v248\n"
    "s_nop 1\n"
    "v_cndmask_b32 v64, v64, v249, s[68:69]\n"
    "v_cndmask_b32 v72, v72, v249, s[68:69]\n"
    "v_cndmask_b32 v65, v65, v249, s[70:71]\n"
    "v_cndmask_b32 v73, v73, v249, s[70:71]\n"
    "v_cndmask_b32 v66, v66, v249, s[72:73]\n"
    "v_cndmask_b32 v74, v74, v249, s[72:73]\n"
    "v_cndmask_b32 v67, v67, v249, s[74:75]\n"
    "v_cndmask_b32 v75, v75, v249, s[74:75]\n"
    "v_cmp_ge_i32 s[68:69], 16, v248\n"
    "v_cmp_ge_i32 s[70:71], 17, v248\n"
    "v_cmp_ge_i32 s[72:73], 18, v248\n"
    "v_cmp_ge_i32 s[74:75], 19, v248\n"
    "s_nop 1\n"
    "v_cndmask_b32 v68, v68, v249, s[68:69]\n"
    "v_cndmask_b32 v76, v76, v249, s[68:69]\n"
    "v_cndmask_b32 v69, v69, v249, s[70:71]\n"
    "v_cndmask_b32 v77, v77, v249, s[70:71]\n"
    "v_cndmask_b32 v70, v70, v249, s[72:73]\n"
    "v_cndmask_b32 v78, v78, v249, s[72:73]\n"
    "v_cndmask_b32 v71, v71, v249, s[74:75]\n"
    "v_cndmask_b32 v79, v79, v249, s[74:75]\n"
    "s_mov_b32 s57, 0\n"
    "s_mov_b32 s56, 0\n"
    "s_branch L2_p1_%=\n"
    "L2_fix1_%=:\n"
    "v_cmp_ge_i32 s[68:69], 0, v248\n"
    "v_cmp_ge_i32 s[70:71], 1, v248\n"
    "v_cmp_ge_i32 s[72:73], 2, v248\n"
    "v_cmp_ge_i32 s[74:75], 3, v248\n"
    "s_nop 1\n"
    "v_cndmask_b32 v64, v64, v249, s[68:69]\n"
    "v_cndmask_b32 v72, v72, v249, s[68:69]\n"
    "v_cndmask_b32 v65, v65, v249, s[70:71]\n"
    "v_cndmask_b32 v73, v73, v249, s[70:71]\n"
    "v_cndmask_b32 v66, v66, v249, s[72:73]\n"
    "v_cndmask_b32 v74, v74, v249, s[72:73]\n"
    "v_cndmask_b32 v67, v67, v249, s[74:75]\n"
    "v_cndmask_b32 v75, v75, v249, s[74:75]\n"
    "v_cmp_ge_i32 s[68:69], 16, v248\n"
    "v_cmp_ge_i32 s[70:71], 17, v248\n"
    "v_cmp_ge_i32 s[72:73], 18, v248\n"
    "v_cmp_ge_i32 s[74:75], 19, v248\n"
    "s_nop 1\n"
    "v_cndmask_b32 v68, v68, v249, s[68:69]\n"
    "v_cndmask_b32 v76, v76, v249, s[68:69]\n"
    "v_cndmask_b32 v69, v69, v249, s[70:71]\n"
    "v_cndmask_b32 v77, v77, v249, s[70:71]\n"
    "v_cndmask_b32 v70, v70, v249, s[72:73]\n"
    "v_cndmask_b32 v78, v78, v249, s[72:73]\n"
    "v_cndmask_b32 v71, v71, v249, s[74:75]\n"
    "v_cndmask_b32 v79, v79, v249, s[74:75]\n"
    "s_mov_b32 s57, 0\n"
    "s_mov_b32 s56, 0\n"
    "s_branch L2_p0_%=\n"
    "L2_ev0_%=:\n"
    "s_cmp_eq_u32 s57, 0\n"
    "s_cbranch_scc0 L2_fix0_%=\n"
    "v_mfma_f32_16x16x32_bf16 v[96:99], a[96:99], v[0:3], v[96:99]\n"
    "s_mov_b32 s61, 0x5f800000\n"
    "ds_read_b64_tr_b16 a[64:65], v225 offset:8192\n"
    "ds_read_b64_tr_b16 a[66:67], v225 offset:12288\n"
    "v_mfma_f32_16x16x32_bf16 v[100:103], a[100:103], v[0:3], v[100:103]\n"
    "s_mov_b64 s[76:77], 0\n"
    "v_exp_f32 v32, v32\n"
    "v_mfma_f32_16x16x32_bf16 v[104:107], a[104:107], v[0:3], v[104:107]\n"
    "ds_read_b64_tr_b16 a[68:69], v225 offset:8448\n"
    "ds_read_b64_tr_b16 a[70:71], v225 offset:12544\n"
    "v_mfma_f32_16x16x32_bf16 v[108:111], a[108:111], v[0:3], v[108:111]\n"
    "v_exp_f32 v33, v33\n"
    "v_mfma_f32_16x16x32_bf16 v[112:115], a[112:115], v[0:3], v[112:115]\n"
    "ds_read_b64_tr_b16 a[72:73], v225 offset:8704\n"
    "ds_read_b64_tr_b16 a[74:75], v225 offset:12800\n"
    "v_mfma_f32_16x16x32_bf16 v[116:119], a[116:119], v[0:3], v[116:119]\n"
    "v_exp_f32 v34, v34\n"
    "v_mfma_f32_16x16x32_bf16 v[120:123], a[120:123], v[0:3], v[120:123]\n"
    "ds_read_b64_tr_b16 a[76:77], v225 offset:8960\n"
    "ds_read_b64_tr_b16 a[78:79], v225 offset:13056\n"
    "v_mfma_f32_16x16x32_bf16 v[124:127], a[124:127], v[0:3], v[124:127]\n"
    "v_exp_f32 v35, v35\n"
    "v_mfma_f32_16x16x32_bf16 a[128:131], a[144:147], v[0:3], a[128:131]\n"
    "ds_read_b64_tr_b16 a[80:81], v225 offset:9216\n"
    "ds_read_b64_tr_b16 a[82:83], v225 offset:13312\n"
    "v_mfma_f32_16x16x32_bf16 v[128:131], a[96:99], v[8:11], v[128:131]\n"
    "v_cvt_pk_bf16_f32 v32, v32, v33\n"
    "v_cvt_pk_bf16_f32 v33, v34, v35\n"
    "v_mfma_f32_16x16x32_bf16 v[132:135], a[100:103], v[8:11], v[132:135]\n"
    "ds_read_b64_tr_b16 a[84:85], v225 offset:9472\n"
    "ds_read_b64_tr_b16 a[86:87], v225 offset:13568\n"
    "v_mfma_f32_16x16x32_bf16 v[136:139], a[104:107], v[8:11], v[136:139]\n"
    "v_exp_f32 v36, v36\n"
    "v_mfma_f32_16x16x32_bf16 v[140:143], a[108:111], v[8:11], v[140:143]\n"
    "ds_read_b64_tr_b16 a[88:89], v225 offset:9728\n"
    "ds_read_b64_tr_b16 a[90:91], v225 offset:13824\n"
    "v_mfma_f32_16x16x32_bf16 v[144:147], a[112:115], v[8:11], v[144:147]\n"
    "v_exp_f32 v37, v37\n"
    "v_mfma_f32_16x16x32_bf16 v[148:151], a[116:119], v[8:11], v[148:151]\n"
    "ds_read_b64_tr_b16 a[92:93], v225 offset:9984\n"
    "ds_read_b64_tr_b16 a[94:95], v225 offset:14080\n"
    "v_mfma_f32_16x16x32_bf16 v[152:155], a[120:123], v[8:11], v[152:155]\n"
    "v_exp_f32 v38, v38\n"
    "v_mfma_f32_16x16x32_bf16 v[156:159], a[124:127], v[8:11], v[156:159]\n"
    "v_exp_f32 v39, v39\n"
    "v_mfma_f32_16x16x32_bf16 a[132:135], a[144:147], v[8:11], a[132:135]\n"
    "v_cvt_pk_bf16_f32 v34, v36, v37\n"
    "v_cvt_pk_bf16_f32 v35, v38, v39\n"
    "s_waitcnt lgkmcnt(0)\n"
    "s_nop 1\n"
    "v_mfma_f32_16x16x32_bf16 a[128:131], a[144:147], v[32:35], a[128:131]\n"
    "v_exp_f32 v40, v40\n"
    "v_mfma_f32_16x16x32_bf16 v[96:99], a[64:67], v[32:35], v[96:99]\n"
    "v_exp_f32 v41, v41\n"
    "v_mfma_f32_16x16x32_bf16 v[100:103], a[68:71], v[32:35], v[100:103]\n"
    "v_exp_f32 v42, v42\n"
    "v_mfma_f32_16x16x32_bf16 v[104:107], a[72:75], v[32:35], v[104:107]\n"
    "v_exp_f32 v43, v43\n"
    "v_mfma_f32_16x16x32_bf16 v[108:111], a[76:79], v[32:35], v[108:111]\n"
    "v_cvt_pk_bf16_f32 v40, v40, v41\n"
    "v_cvt_pk_bf16_f32 v41, v42, v43\n"
    "v_mfma_f32_16x16x32_bf16 v[112:115], a[80:83], v[32:35], v[112:115]\n"
    "v_exp_f32 v44, v44\n"
    "v_mfma_f32_16x16x32_bf16 v[116:119], a[84:87], v[32:35], v[116:119]\n"
    "v_exp_f32 v45, v45\n"
    "v_mfma_f32_16x16x32_bf16 v[120:123], a[88:91], v[32:35], v[120:123]\n"
    "v_exp_f32 v46, v46\n"
    "v_mfma_f32_16x16x32_bf16 v[124:127], a[92:95], v[32:35], v[124:127]\n"
    "v_exp_f32 v47, v47\n"
    "s_nop 0\n"
    "v_cvt_pk_bf16_f32 v42, v44, v45\n"
    "s_nop 0\n"
    "v_cvt_pk_bf16_f32 v43, v46, v47\n"
    "s_nop 1\n"
    "v_mfma_f32_16x16x32_bf16 a[132:135], a[144:147], v[40:43], a[132:135]\n"
    "v_accvgpr_read_b32 v88, a128\n"
    "v_cmp_nge_f32 vcc, s61, v88\n"
    "v_mfma_f32_16x16x32_bf16 v[128:131], a[64:67], v[40:43], v[128:131]\n"
    "v_rcp_f32 v64, v88\n"
    "v_mfma_f32_16x16x32_bf16 v[132:135], a[68:71], v[40:43], v[132:135]\n"
    "s_or_b64 s[76:77], s[76:77], vcc\n"
    "v_mov_b32 v65, v64\n"
    "v_mfma_f32_16x16x32_bf16 v[136:139], a[72:75], v[40:43], v[136:139]\n"
    "v_mul_f32 v96, v96, v64\n"
    "v_mul_f32 v97, v97, v64\n"
    "v_mfma_f32_16x16x32_bf16 v[140:143], a[76:79], v[40:43], v[140:143]\n"
    "v_mul_f32 v98, v98, v64\n"
    "v_mul_f32 v99, v99, v64\n"
    "v_mfma_f32_16x16x32_bf16 v[144:147], a[80:83], v[40:43], v[144:147]\n"
    "v_cvt_pk_bf16_f32 v96, v96, v97\n"
    "v_cvt_pk_bf16_f32 v97, v98, v99\n"
    "v_mfma_f32_16x16x32_bf16 v[148:151], a[84:87], v[40:43], v[148:151]\n"
    "ds_write_b64 v251, v[96:97] offset:0\n"
    "v_mul_f32 v100, v100, v64\n"
    "v_mfma_f32_16x16x32_bf16 v[152:155], a[88:91], v[40:43], v[152:155]\n"
    "v_mul_f32 v101, v101, v64\n"
    "v_mul_f32 v102, v102, v64\n"
    "v_mfma_f32_16x16x32_bf16 v[156:159], a[92:95], v[40:43], v[156:159]\n"
    "v_mul_f32 v103, v103, v64\n"
    "v_cvt_pk_bf16_f32 v100, v100, v101\n"
    "v_cvt_pk_bf16_f32 v101, v102, v103\n"
    "v_pk_mul_f32 v[104:105], v[104:105], v[64:65]\n"
    "v_pk_mul_f32 v[106:107], v[106:107], v[64:65]\n"
    "v_cvt_pk_bf16_f32 v104, v104, v105\n"
    "v_cvt_pk_bf16_f32 v105, v106, v107\n"
    "v_pk_mul_f32 v[108:109], v[108:109], v[64:65]\n"
    "v_pk_mul_f32 v[110:111], v[110:111], v[64:65]\n"
    "v_cvt_pk_bf16_f32 v108, v108, v109\n"
    "v_cvt_pk_bf16_f32 v109, v110, v111\n"
    "ds_write_b64 v251, v[100:101] offset:32\n"
    "ds_write_b64 v251, v[104:105] offset:64\n"
    "ds_write_b64 v251, v[108:109] offset:96\n"
    "v_pk_mul_f32 v[112:113], v[112:113], v[64:65]\n"
    "v_pk_mul_f32 v[114:115], v[114:115], v[64:65]\n"
    "v_cvt_pk_bf16_f32 v112, v112, v113\n"
    "v_cvt_pk_bf16_f32 v113, v114, v115\n"
    "v_pk_mul_f32 v[116:117], v[116:117], v[64:65]\n"
    "v_pk_mul_f32 v[118:119], v[118:119], v[64:65]\n"
    "v_cvt_pk_bf16_f32 v116, v116, v117\n"
    "v_cvt_pk_bf16_f32 v117, v118, v119\n"
    "ds_write_b64 v251, v[112:113] offset:128\n"
    "ds_write_b64 v251, v[116:117] offset:160\n"
    "v_pk_mul_f32 v[120:121], v[120:121], v[64:65]\n"
    "v_pk_mul_f32 v[122:123], v[122:123], v[64:65]\n"
    "v_cvt_pk_bf16_f32 v120, v120, v121\n"
    "v_cvt_pk_bf16_f32 v121, v122, v123\n"
    "v_pk_mul_f32 v[124:125], v[124:125], v[64:65]\n"
    "v_pk_mul_f32 v[126:127], v[126:127], v[64:65]\n"
    "v_cvt_pk_bf16_f32 v124, v124, v125\n"
    "v_cvt_pk_bf16_f32 v125, v126, v127\n"
    "ds_write_b64 v251, v[120:121] offset:192\n"
    "ds_write_b64 v251, v[124:125] offset:224\n"
    "ds_read_b128 v[0:3], v252 offset:0\n"
    "ds_read_b128 v[4:7], v252 offset:1088\n"
    "ds_read_b128 v[8:11], v252 offset:2176\n"
    "ds_read_b128 v[12:15], v252 offset:3264\n"
    "v_accvgpr_read_b32 v89, a132\n"
    "v_cmp_nge_f32 vcc, s61, v89\n"
    "v_rcp_f32 v66, v89\n"
    "s_or_b64 s[76:77], s[76:77], vcc\n"
    "v_mov_b32 v67, v66\n"
    "v_pk_mul_f32 v[128:129], v[128:129], v[66:67]\n"
    "v_pk_mul_f32 v[130:131], v[130:131], v[66:67]\n"
    "v_cvt_pk_bf16_f32 v128, v128, v129\n"
    "v_cvt_pk_bf16_f32 v129, v130, v131\n"
    "v_pk_mul_f32 v[132:133], v[132:133], v[66:67]\n"
    "v_pk_mul_f32 v[134:135], v[134:135], v[66:67]\n"
    "v_cvt_pk_bf16_f32 v132, v132, v133\n"
    "v_cvt_pk_bf16_f32 v133, v134, v135\n"
    "s_mov_b32 s53, 0\n"
    "s_waitcnt lgkmcnt(3)\n"
    "buffer_store_dwordx4 v[0:3], v253, s[48:51], s53 offen nt\n"
    "ds_write_b64 v251, v[128:129] offset:4352\n"
    "ds_write_b64 v251, v[132:133] offset:4384\n"
    "v_pk_mul_f32 v[136:137], v[136:137], v[66:67]\n"
    "v_pk_mul_f32 v[138:139], v[138:139], v[66:67]\n"
    "v_cvt_pk_bf16_f32 v136, v136, v137\n"
    "v_cvt_pk_bf16_f32 v137, v138, v139\n"
    "v_pk_mul_f32 v[140:141], v[140:141], v[66:67]\n"
    "v_pk_mul_f32 v[142:143], v[142:143], v[66:67]\n"
    "v_cvt_pk_bf16_f32 v140, v140, v141\n"
    "v_cvt_pk_bf16_f32 v141, v142, v143\n"
    "s_mov_b32 s53, 16384\n"
    "s_waitcnt lgkmcnt(4)\n"
    "buffer_store_dwordx4 v[4:7], v253, s[48:51], s53 offen nt\n"
    "ds_write_b64 v251, v[136:137] offset:4416\n"
    "ds_write_b64 v251, v[140:141] offset:4448\n"
    "v_pk_mul_f32 v[144:145], v[144:145], v[66:67]\n"
    "v_pk_mul_f32 v[146:147], v[146:147], v[66:67]\n"
    "v_cvt_pk_bf16_f32 v144, v144, v145\n"
    "v_cvt_pk_bf16_f32 v145, v146, v147\n"
    "v_pk_mul_f32 v[148:149], v[148:149], v[66:67]\n"
    "v_pk_mul_f32 v[150:151], v[150:151], v[66:67]\n"
    "v_cvt_pk_bf16_f32 v148, v148, v149\n"
    "v_cvt_pk_bf16_f32 v149, v150, v151\n"
    "s_mov_b32 s53, 32768\n"
    "s_waitcnt lgkmcnt(5)\n"
    "buffer_store_dwordx4 v[8:11], v253, s[48:51], s53 offen nt\n"
    "ds_write_b64 v251, v[144:145] offset:4480\n"
    "ds_write_b64 v251, v[148:149] offset:4512\n"
    "v_pk_mul_f32 v[152:153], v[152:153], v[66:67]\n"
    "v_pk_mul_f32 v[154:155], v[154:155], v[66:67]\n"
    "v_cvt_pk_bf16_f32 v152, v152, v153\n"
    "v_cvt_pk_bf16_f32 v153, v154, v155\n"
    "v_pk_mul_f32 v[156:157], v[156:157], v[66:67]\n"
    "v_pk_mul_f32 v[158:159], v[158:159], v[66:67]\n"
    "v_cvt_pk_bf16_f32 v156, v156, v157\n"
    "v_cvt_pk_bf16_f32 v157, v158, v159\n"
    "s_mov_b32 s53, 49152\n"
    "s_waitcnt lgkmcnt(6)\n"
    "buffer_store_dwordx4 v[12:15], v253, s[48:51], s53 offen nt\n"
    "ds_write_b64 v251, v[152:153] offset:4544\n"
    "ds_write_b64 v251, v[156:157] offset:4576\n"
    "ds_read_b128 v[16:19], v252 offset:4352\n"
    "ds_read_b128 v[20:23], v252 offset:5440\n"
    "ds_read_b128 v[24:27], v252 offset:6528\n"
    "ds_read_b128 v[28:31], v252 offset:7616\n"
    "s_mov_b32 s53, 65536\n"
    "s_waitcnt lgkmcnt(3)\n"
    "buffer_store_dwordx4 v[16:19], v253, s[48:51], s53 offen nt\n"
    "s_mov_b32 s53, 81920\n"
    "s_waitcnt lgkmcnt(2)\n"
    "buffer_store_dwordx4 v[20:23], v253, s[48:51], s53 offen nt\n"
    "s_mov_b32 s53, 98304\n"
    "s_waitcnt lgkmcnt(1)\n"
    "buffer_store_dwordx4 v[24:27], v253, s[48:51], s53 offen nt\n"
    "s_mov_b32 s53, 114688\n"
    "s_waitcnt lgkmcnt(0)\n"
    "buffer_store_dwordx4 v[28:31], v253, s[48:51], s53 offen nt\n"
    "s_or_b32 %0, s76, s77\n"
    "L2_end_%=:\n"
    "Lk_end2:\n"
    "s_nop 1\n"
    : "=&s"(flag) : "s"(kb), "s"(nrk), "s"(vb), "s"(qp), "s"(nrq), "s"(op), "s"(nh), "s"(nv), "s"(w), "s"(ldsb), "s"(nv0), "a"(k0), "a"(k1)
    : "v0", "v1", "v2", "v3", "v4", "v5", "v6", "v7", "v8", "v9", "v10", "v11", "v12", "v13", "v14", "v15", "v16", "v17", "v18", "v19", "v20", "v21", "v22", "v23", "v24", "v25", "v26", "v27", "v28", "v29", "v30", "v31", "v32", "v33", "v34", "v35", "v36", "v37", "v38", "v39", "v40", "v41", "v42", "v43", "v44", "v45", "v46", "v47", "v48", "v49", "v50", "v51", "v52", "v53", "v54", "v55", "v56", "v57", "v58", "v59", "v60", "v61", "v62", "v63", "v64", "v65", "v66", "v67", "v68", "v69", "v70", "v71", "v72", "v73", "v74", "v75", "v76", "v77", "v78", "v79", "v80", "v81", "v82", "v83", "v84", "v85", "v86", "v87", "v88", "v89", "v90", "v91", "v92", "v93", "v94", "v95", "v96", "v97", "v98", "v99", "v100", "v101", "v102", "v103", "v104", "v105", "v106", "v107", "v108", "v109", "v110", "v111", "v112", "v113", "v114", "v115", "v116", "v117", "v118", "v119", "v120", "v121", "v122", "v123", "v124", "v125", "v126", "v127", "v128", "v129", "v130", "v131", "v132", "v133", "v134", "v135", "v136", "v137", "v138", "v139", "v140", "v141", "v142", "v143", "v144", "v145", "v146", "v147", "v148", "v149", "v150", "v151", "v152", "v153", "v154", "v155", "v156", "v157", "v158", "v159", "v160", "v161", "v162", "v163", "v164", "v165", "v166", "v167", "v168", "v169", "v170", "v171", "v172", "v173", "v174", "v175", "v176", "v177", "v178", "v179", "v180", "v181", "v182", "v183", "v184", "v185", "v186", "v187", "v188", "v189", "v190", "v191", "v192", "v193", "v194", "v195", "v196", "v197", "v198", "v199", "v200", "v201", "v202", "v203", "v204", "v205", "v206", "v207", "v208", "v209", "v210", "v211", "v212", "v213", "v214", "v215", "v216", "v217", "v218", "v219", "v220", "v221", "v222", "v223", "v224", "v225", "v226", "v227", "v228", "v229", "v230", "v231", "v232", "v233", "v234", "v235", "v236", "v237", "v238", "v239", "v240", "v241", "v242", "v243", "v244", "v245", "v246", "v247", "v248", "v249", "v250", "v251", "v252", "v253", "a0", "a1", "a2", "a3", "a4", "a5", "a6", "a7", "a8", "a9", "a10", "a11", "a12", "a13", "a14", "a15", "a16", "a17", "a18", "a19", "a20", "a21", "a22", "a23", "a24", "a25", "a26", "a27", "a28", "a29", "a30", "a31", "a32", "a33", "a34", "a35", "a36", "a37", "a38", "a39", "a40", "a41", "a42", "a43", "a44", "a45", "a46", "a47", "a48", "a49", "a50", "a51", "a52", "a53", "a54", "a55", "a56", "a57", "a58", "a59", "a60", "a61", "a62", "a63", "a64", "a65", "a66", "a67", "a68", "a69", "a70", "a71", "a72", "a73", "a74", "a75", "a76", "a77", "a78", "a79", "a80", "a81", "a82", "a83", "a84", "a85", "a86", "a87", "a88", "a89", "a90", "a91", "a92", "a93", "a94", "a95", "a96", "a97", "a98", "a99", "a100", "a101", "a102", "a103", "a104", "a105", "a106", "a107", "a108", "a109", "a110", "a111", "a112", "a113", "a114", "a115", "a116", "a117", "a118", "a119", "a120", "a121", "a122", "a123", "a124", "a125", "a126", "a127", "a128", "a129", "a130", "a131", "a132", "a133", "a134", "a135", "a136", "a137", "a138", "a139", "a140", "a141", "a142", "a143", "a144", "a145", "a146", "a147", "a148", "a152", "a153", "a154", "a155", "a156", "a157", "a158", "a159", "a160", "a161", "a162", "a163", "a164", "a165", "a166", "a167", "a168", "a169", "a170", "a171", "a172", "a173", "a174", "a175", "a176", "a177", "a178", "a179", "a180", "a181", "a182", "a183", "a184", "a185", "a186", "a187", "a188", "a189", "a190", "a191", "a192", "a193", "a194", "a195", "s36", "s37", "s38", "s39", "s40", "s41", "s42", "s43", "s44", "s45", "s46", "s47", "s48", "s49", "s50", "s51", "s52", "s53", "s54", "s55", "s56", "s57", "s58", "s59", "s60", "s61", "s62", "s63", "s64", "s65", "s66", "s67", "s68", "s69", "s70", "s71", "s72", "s73", "s74", "s75", "s76", "s77", "vcc", "scc", "memory");
  return flag;
}

typedef short bf16x8 __attribute__((ext_vector_type(8)));
typedef short s16x4 __attribute__((ext_vector_type(4)));
typedef float f32x4 __attribute__((ext_vector_type(4)));
typedef unsigned u32x4 __attribute__((ext_vector_type(4)));
typedef unsigned u32x2 __attribute__((ext_vector_type(2)));
typedef __bf16 bf2_t __attribute__((ext_vector_type(2)));
typedef float f32x2 __attribute__((ext_vector_type(2)));
__device__ __forceinline__ unsigned pk2(float lo, float hi) { f32x2 x = {lo, hi}; return __builtin_bit_cast(unsigned, __builtin_convertvector(x, bf2_t)); }

__device__ __forceinline__ void slow_rows(const char* __restrict__ Qr, const char* __restrict__ Kh, const char* __restrict__ Vh, char* __restrict__ Or,
                                          int nrows, int L, char* ldsw) {
  int lane; asm volatile("v_mbcnt_lo_u32_b32 %0, -1, 0\n\tv_mbcnt_hi_u32_b32 %0, -1, %0" : "=v"(lane));
  const int i16 = lane & 15, g = lane >> 4;
  const float C = 0.12751743082f;
  for (int q0 = 0; q0 < nrows; q0 += 16) {
    const int qrow = min(q0 + i16, nrows - 1);
    bf16x8 qf[4];
#pragma unroll
    for (int s = 0; s < 4; ++s) qf[s] = *(const bf16x8*)(Qr + (size_t)qrow * 4096 + 64 * s + 16 * g);
    float m = -3.0e38f, l = 0.f; f32x4 o[8];
#pragma unroll
    for (int d = 0; d < 8; ++d) o[d] = f32x4{0.f, 0.f, 0.f, 0.f};
    for (int k0 = 0; k0 < L; k0 += 32) {
      f32x4 sc[2];
#pragma unroll
      for (int kb = 0; kb < 2; ++kb) { sc[kb] = f32x4{0.f, 0.f, 0.f, 0.f}; const int kr = min(k0 + 16 * kb + i16, L - 1);
#pragma unroll
        for (int s = 0; s < 4; ++s) { const bf16x8 kf = *(const bf16x8*)(Kh + (size_t)kr * 4096 + 64 * s + 16 * g);
          sc[kb] = __builtin_amdgcn_mfma_f32_16x16x32_bf16(kf, qf[s], sc[kb], 0, 0, 0); } }
#pragma unroll
      for (int j = 0; j < 8; ++j) { const int key = 4 * j + g, ch = i16; u32x4 x = {0u, 0u, 0u, 0u};
        if (k0 + key < L) x = *(const u32x4*)(Vh + (size_t)(k0 + key) * 4096 + 16 * ch);
        *(u32x4*)(ldsw + 2048 * (key >> 3) + 256 * (ch >> 1) + 32 * (key & 7) + 16 * (ch & 1)) = x; }
      float tm = -__builtin_inff();
#pragma unroll
      for (int kb = 0; kb < 2; ++kb)
#pragma unroll
        for (int r = 0; r < 4; ++r) { if (k0 + 16 * kb + 4 * g + r >= L) sc[kb][r] = -__builtin_inff(); tm = fmaxf(tm, sc[kb][r]); }
      tm = fmaxf(tm, __shfl_xor(tm, 16)); tm = fmaxf(tm, __shfl_xor(tm, 32));
      const float mn = fmaxf(m, tm), al = __builtin_amdgcn_exp2f((m - mn) * C); m = mn;
      float ps = 0.f;
#pragma unroll
      for (int kb = 0; kb < 2; ++kb)
#pragma unroll
        for (int r = 0; r < 4; ++r) { sc[kb][r] = __builtin_amdgcn_exp2f((sc[kb][r] - mn) * C); ps += sc[kb][r]; }
      l = l * al + ps;
      const u32x4 pw = {pk2(sc[0][0], sc[0][1]), pk2(sc[0][2], sc[0][3]), pk2(sc[1][0], sc[1][1]), pk2(sc[1][2], sc[1][3])};
      const bf16x8 pf = __builtin_bit_cast(bf16x8, pw);
      __builtin_amdgcn_s_waitcnt(0xC07F);
      __builtin_amdgcn_wave_barrier();
      typedef __attribute__((address_space(3))) s16x4* lp_t;
      const unsigned vb = (unsigned)(uintptr_t)ldsw + 2048 * (g >> 1) + 8 * (lane & 31);
#pragma unroll
      for (int d = 0; d < 8; ++d) {
        const s16x4 lo = __builtin_amdgcn_ds_read_tr16_b64_v4i16((lp_t)(uintptr_t)(vb + 256 * d));
        const s16x4 hi = __builtin_amdgcn_ds_read_tr16_b64_v4i16((lp_t)(uintptr_t)(vb + 4096 + 256 * d));
        const bf16x8 vf = {lo[0], lo[1], lo[2], lo[3], hi[0], hi[1], hi[2], hi[3]};
        o[d] = o[d] * al;
        o[d] = __builtin_amdgcn_mfma_f32_16x16x32_bf16(vf, pf, o[d], 0, 0, 0);
      }
      __builtin_amdgcn_s_waitcnt(0xC07F);
      __builtin_amdgcn_wave_barrier();
    }
    l += __shfl_xor(l, 16); l += __shfl_xor(l, 32);
    const float il = 1.0f / l;
    if (q0 + i16 < nrows) {
#pragma unroll
      for (int d = 0; d < 8; ++d) { const u32x2 w2 = {pk2(o[d][0] * il, o[d][1] * il), pk2(o[d][2] * il, o[d][3] * il)};
        *(u32x2*)(Or + (size_t)(q0 + i16) * 4096 + 32 * d + 8 * g) = w2; }
    }
  }
}

constexpr float CA = 253.f, CB = 105.f, CO = 7000.f;
__device__ __forceinline__ float fdivfloor(float a, float b) { return floorf((a + 0.5f) * __builtin_amdgcn_rcpf(b)); }
__device__ __forceinline__ float wg_cost(float k, float nh) { const float ns = floorf((k + 3.f) * 0.25f); return nh * (CA * fmaxf(k, 2.f) + CB * ns) + CO * ns; }

__global__ __launch_bounds__(256, 1)
void attn_fwd(const char* __restrict__ Q, const char* __restrict__ K, const char* __restrict__ V, const int* __restrict__ cu, char* __restrict__ O, int nseq) {
  extern __shared__ __attribute__((aligned(1024))) char lds[];
  const int bid = blockIdx.x;
  const int h = 2 * (bid & 7) + ((bid >> 3) & 1);
  const int gi = bid >> 4;
  const unsigned w = __builtin_amdgcn_readfirstlane(threadIdx.x >> 6);
  const int lane = threadIdx.x & 63;
  const unsigned ldsb = (unsigned)(uintptr_t)lds;
  unsigned pfd = 0, pq0 = 0;
#define PREFETCH_CODE() { unsigned long long pc; unsigned sz, t; const unsigned off = threadIdx.x * 128u; \
    asm volatile("s_getpc_b64 %1\nLk_begin:\n\ts_mov_b32 %2, Lk_end4-Lk_begin\n\ts_max_u32 %2, %2, Lk_end3-Lk_begin\n\ts_max_u32 %2, %2, Lk_end2-Lk_begin\n\t" \
                 "v_min_u32 %3, %2, %5\n\tglobal_load_dword %0, %3, %1\n\tv_add_u32 %3, 0x8000, %5\n\tv_min_u32 %3, %2, %3\n\tglobal_load_dword %4, %3, %1" : "=&a"(pfd), "=&s"(pc), "=&s"(sz), "=&v"(t), "=&a"(pq0) : "v"(off) : "scc"); }
  int my_s0 = 0, my_L = 0, g_lo = 0, g_hi = 0; const bool planned = nseq <= GPH;
  if (planned) {
    const int lgF = nseq <= 4 ? 4 : (nseq <= 8 ? 3 : 2);
    const int sl = lane >> lgF, il = lane & ((1 << lgF) - 1);
    int c0, c1;
    asm volatile("global_load_dword %0, %2, %4\n\tglobal_load_dword %1, %3, %4" : "=&v"(c0), "=&v"(c1) : "v"(4 * min(sl, nseq)), "v"(4 * min(sl + 1, nseq)), "s"(cu));
    PREFETCH_CODE();
    asm volatile("s_waitcnt vmcnt(2)" : "+v"(c0), "+v"(c1));
    const int Ll = c1 - c0;
    const float nh = (float)((Ll + 31) >> 5), gr = (float)((Ll + 63) >> 6), fi = (float)il;
    const float kk = fdivfloor(gr + fi - 1.f, fi);
    unsigned key = __builtin_bit_cast(unsigned, wg_cost(kk, nh)) >> 16;
    if (il == 0) key = 0xFFFFu;
    if (Ll <= 0 || fi >= gr) key = 0u;
    unsigned lo = 0x4400u;
    { unsigned t, c;
#define BIS(x) "s_add_u32 %1, %0, " #x "\n\tv_cmp_lt_u32 vcc, %1, %3\n\ts_bcnt1_i32_b64 %2, vcc\n\ts_cmp_gt_u32 %2, 16\n\ts_cselect_b32 %0, %1, %0\n\t"
      asm volatile(BIS(2048) BIS(1024) BIS(512) BIS(256) BIS(128) BIS(64) BIS(32) BIS(16) BIS(8) BIS(4) BIS(2) BIS(1)
                   : "+s"(lo), "=&s"(t), "=&s"(c) : "v"(key) : "vcc", "scc");
#undef BIS
      static_assert(GPH == 16, "bisection constant"); }
    const unsigned long long need = __ballot(key > lo + 1u);
    const int rank = __builtin_amdgcn_mbcnt_hi((unsigned)(need >> 32), __builtin_amdgcn_mbcnt_lo((unsigned)need, 0u));
    const unsigned long long me = __ballot(key > lo + 1u && rank == gi);
    if (me) {
      const int ml = __builtin_ctzll(me), fo = ml & ((1 << lgF) - 1);
      const unsigned fld = (unsigned)(need >> (ml - fo)) & ((1u << (1 << lgF)) - 1u);
      const int Ws = __builtin_popcount(fld), i = __builtin_popcount(fld & ((1u << fo) - 1u));
      const float rW = (float)Ws;
      const int qnl = (int)fdivfloor(gr, rW);
      const int qn = __builtin_amdgcn_readlane(qnl, ml);
      my_s0 = __builtin_amdgcn_readlane(c0, ml); my_L = __builtin_amdgcn_readlane(Ll, ml);
      const int rm = ((my_L + 63) >> 6) - qn * Ws;
      g_lo = i * qn + min(i, rm); g_hi = g_lo + qn + (i < rm ? 1 : 0);
    }
  }
  bool first = true;
  int seq = 0, item = gi;
  for (;;) {
    int s0, L, r0, n;
    if (planned) {
      const int k = g_hi - g_lo; if (k <= 0) break;
      n = k <= 4 ? k : (k <= 6 ? 3 : (k == 9 ? 3 : 4));
      s0 = my_s0; L = my_L; r0 = g_lo * 64; g_lo += n;
    } else {
      bool found = false; s0 = 0; L = 0;
      while (seq < nseq) { const int a = cu[seq], b = cu[seq + 1]; const int nb = (b - a + 255) >> 8; if (item < nb) { s0 = a; L = b - a; found = true; break; } item -= nb; ++seq; }
      if (!found) break;
      r0 = item * 256; n = min(4, (L - r0 + 63) >> 6); item += GPH;
    }
    const int rows = min(64 * n, L - r0);
    const int mode = n < 2 ? 2 : n;
    const size_t hoff = (size_t)h * (D * 2);
    const uint64_t kb = (uint64_t)(K + (size_t)s0 * 4096 + hoff - 4096);
    const uint64_t vb = (uint64_t)(V + (size_t)s0 * 4096 + hoff - 4096);
    const unsigned nrk = (unsigned)(L - 1) * 4096u + 256u + 4096u;
    const uint64_t qp = (uint64_t)(Q + (size_t)(s0 + r0) * 4096 + hoff);
    const uint64_t op = (uint64_t)(O + (size_t)(s0 + r0) * 4096 + hoff);
    const unsigned nrq = (unsigned)(rows - 1) * 4096u + 256u;
    const unsigned nh = max((unsigned)(L + 31) >> 5, 2u);
    const unsigned nv = (unsigned)max(L - 32 * (int)(nh - 1), 0), nv0 = (unsigned)min(L, 32);
    unsigned flag;
    if (!first) asm volatile("s_waitcnt vmcnt(0) lgkmcnt(0)\n\ts_barrier" ::: "memory");
    first = false;
    if (mode == 4) flag = seg4(kb, nrk, vb, qp, nrq, op, nh, nv, w, ldsb, nv0, pfd, pq0);
    else if (mode == 3) flag = seg3(kb, nrk, vb, qp, nrq, op, nh, nv, w, ldsb, nv0, pfd, pq0);
    else flag = seg2(kb, nrk, vb, qp, nrq, op, nh, nv, w, ldsb, nv0, pfd, pq0);
    if (__builtin_expect(flag != 0, 0)) {
      asm volatile("s_waitcnt vmcnt(0) lgkmcnt(0)" ::: "memory");
      const int wr0 = (int)w * 16 * mode, wn = min(16 * mode, rows - wr0);
      if (wn > 0) slow_rows((const char*)qp + (size_t)wr0 * 4096, (const char*)kb + 4096, (const char*)vb + 4096, (char*)op + (size_t)wr0 * 4096, wn, L, lds + LDS_BYTES + w * 16384);
    }
  }
}
extern "C" void kernel_launch(void* const* d_in, const int* in_sizes, int n_in, void* d_out, int out_size, void* d_ws, size_t ws_size, hipStream_t stream) {
  const int nseq = in_sizes[3] - 1;
  if (nseq <= 0 || in_sizes[0] < LDE) return;
  (void)hipFuncSetAttribute((const void*)attn_fwd, hipFuncAttributeMaxDynamicSharedMemorySize, LDS_BYTES + 65536);
  hipLaunchKernelGGL(attn_fwd, dim3(NWG), dim3(256), LDS_BYTES + 65536, stream, (const char*)d_in[0], (const char*)d_in[1], (const char*)d_in[2], (const int*)d_in[3], (char*)d_out, nseq);
}
```
